```python
import torch

def _softmax(x, axis=-1):
    return torch.softmax(x, dim=axis)
def _gelu(x):
    return torch.nn.functional.gelu(x, approximate="tanh")
def _silu(x):
    return torch.nn.functional.silu(x)
def _sigmoid(x):
    return torch.sigmoid(x)
def _layernorm(x, axis=-1, eps=1e-5):
    m = x.mean(dim=axis, keepdim=True)
    v = x.var(dim=axis, keepdim=True, unbiased=False)
    return (x - m) / torch.sqrt(v + eps)
def _scatter_add(tgt, idx, src, axis=0):
    return tgt.clone().index_add_(axis, idx, src)
def _segment_sum(src, seg, n):
    out = torch.zeros((n,) + src.shape[1:], dtype=src.dtype)
    return out.index_add_(0, seg, src)
def _pad(x, pw):
    flat = tuple(v for pair in reversed(pw) for v in pair)
    return torch.nn.functional.pad(x, flat)

kernel_name = 'sequential_preLN__spectral_gfnet__moe_arch_mod__prompt'

def setup_inputs():

    _w = (lambda a: (lambda t: t.to(torch.bfloat16) if t.is_floating_point() else t)(torch.as_tensor(a)))
    def _A_setup():
        B, S, D = 1, 2048, 1024
        Df = D // 2 + 1
        return {
            "x": _w((lambda *s: torch.randn(*s, dtype=torch.bfloat16) * 0.05)(B, S, D)),
            "w_r": _w((lambda *s: torch.randn(*s, dtype=torch.bfloat16) * 1.6)(S, Df) * 0.02),
            "w_i": _w((lambda *s: torch.randn(*s, dtype=torch.bfloat16) * 1.6)(S, Df) * 0.02),
        }
    _A_in = _A_setup()

    def _B_setup():
        B, S, D, F, C = 1, 2048, 1024, 3584, 8
        return {
            "x": _w((lambda *s: torch.randn(*s, dtype=torch.bfloat16) * 0.05)(B, S, D)),
            "w_r": _w((lambda *s: torch.randn(*s, dtype=torch.bfloat16) * 0.2)(D)),
            "w_gate": _w((lambda *s: torch.randn(*s, dtype=torch.bfloat16) * 0.2)(D, F)),
            "w_up": _w((lambda *s: torch.randn(*s, dtype=torch.bfloat16) * 0.2)(D, F)),
            "w_down": _w((lambda *s: torch.randn(*s, dtype=torch.bfloat16) * 0.2)(F, D)),
        }
    _B_in = _B_setup()

    return {
        "A_x": _A_in["x"],
        "A_w_r": _A_in["w_r"],
        "A_w_i": _A_in["w_i"],
        "B_x": _B_in["x"],
        "B_w_r": _B_in["w_r"],
        "B_w_gate": _B_in["w_gate"],
        "B_w_up": _B_in["w_up"],
        "B_w_down": _B_in["w_down"],
    }

def reference(A_x, A_w_r, A_w_i, B_x, B_w_r, B_w_gate, B_w_up, B_w_down):
    def _A_ref(x, w_r, w_i):
        B, S, D = x.shape
        X = (lambda x, axes=(-2,-1): torch.fft.rfft2(x.float(), dim=axes))(x, axes=(-2, -1))
        W = w_r + 1j * w_i
        X = X * W
        y = (lambda x, s, axes=(-2,-1): torch.fft.irfft2(x, s=s, dim=axes).to(torch.bfloat16))(X, (S, D), axes=(-2, -1))
        return y

    def _B_ref(x, w_r, w_gate, w_up, w_down):
        B, S, D = x.shape
        C = 8
        r = x @ w_r
        idx = torch.argsort(-r, axis=-1)[:, :C]
        idx_e = (lambda x, n, axis: torch.repeat_interleave(x, n, dim=axis))(idx.reshape(B, C, 1), D, axis=2)
        x_sel = (lambda x, i, axis: torch.take_along_dim(x, i, dim=axis))(x, idx_e, axis=1)
        g = _silu(x_sel @ w_gate)
        u = x_sel @ w_up
        y = (g * u) @ w_down
        r_sel = _sigmoid((lambda x, i, axis: torch.take_along_dim(x, i, dim=axis))(r, idx, axis=1))
        y = y * r_sel.reshape(B, C, 1)
        b_off = (torch.arange(B) * S).reshape(B, 1)
        flat_idx = (idx + b_off).reshape(B * C)
        zero = (x * 0.0).reshape(B * S, D)
        upd = _scatter_add(zero, flat_idx, y.reshape(B * C, D), axis=0)
        return x + upd.reshape(B, S, D)

    x = A_x
    h = x + _A_ref(_layernorm(x, axis=-1), A_w_r, A_w_i)
    h2 = h + _B_ref(_layernorm(h, axis=-1), B_w_r, B_w_gate, B_w_up, B_w_down)
    return h2
```

```cpp
#include <hip/hip_runtime.h>
#include <hip/hip_bf16.h>
#include <stdint.h>
typedef unsigned short u16;
#define DI __device__ __forceinline__
static constexpr int S = 2048, D = 1024, DF = 513, F = 3584, C = 8;
DI float bf2f(u16 v) { return __uint_as_float(((unsigned)v) << 16); }
DI u16 f2bf(float f) { unsigned u = __float_as_uint(f); u += 0x7fffu + ((u >> 16) & 1u); return (u16)(u >> 16); }
DI float rbf(float f) { return bf2f(f2bf(f)); }
DI float block_sum(float v, float* red) {
    for (int o = 32; o; o >>= 1) v += __shfl_xor(v, o);
    __syncthreads();
    if ((threadIdx.x & 63) == 0) red[threadIdx.x >> 6] = v;
    __syncthreads();
    float s = 0; for (int i = 0; i < (int)(blockDim.x >> 6); ++i) s += red[i];
    return s;
}
template <int N, int SIGN> DI float2* fft_lds(float2* a, float2* b) {
    for (int Ns = 1; Ns < N; Ns <<= 1) {
        __syncthreads();
        for (int j = threadIdx.x; j < N / 2; j += 256) {
            int k = j & (Ns - 1);
            float sn, cs; sincospif((float)SIGN * (float)k / (float)Ns, &sn, &cs);
            float2 u = a[j], v = a[j + N / 2];
            float2 t = make_float2(v.x * cs - v.y * sn, v.x * sn + v.y * cs);
            int o = ((j - k) << 1) + k;
            b[o] = make_float2(u.x + t.x, u.y + t.y);
            b[o + Ns] = make_float2(u.x - t.x, u.y - t.y);
        }
        float2* t = a; a = b; b = t;
    }
    __syncthreads();
    return a;
}
__global__ __launch_bounds__(256) void k1(const u16* x, float2* R) {
    __shared__ float2 A[D], B[D]; __shared__ float red[4];
    int s = blockIdx.x; float v[4]; float sum = 0;
    for (int i = 0; i < 4; ++i) { v[i] = bf2f(x[s * D + threadIdx.x + 256 * i]); sum += v[i]; }
    float mean = block_sum(sum, red) / D; float q = 0;
    for (int i = 0; i < 4; ++i) { float d = v[i] - mean; q += d * d; }
    float var = rbf(block_sum(q, red) / D); float m = rbf(mean);
    float sd = rbf(sqrtf(rbf(var + 1e-5f)));
    for (int i = 0; i < 4; ++i) A[threadIdx.x + 256 * i] = make_float2(rbf(rbf(v[i] - m) / sd), 0.f);
    float2* r = fft_lds<D, -1>(A, B);
    for (int k = threadIdx.x; k < DF; k += 256) R[(size_t)s * DF + k] = r[k];
}
__global__ __launch_bounds__(256) void k2(float2* R, const u16* wr, const u16* wi) {
    __shared__ float2 A[S], B[S];
    int k = blockIdx.x;
    for (int s = threadIdx.x; s < S; s += 256) A[s] = R[(size_t)s * DF + k];
    float2* r = fft_lds<S, -1>(A, B); float2* o = (r == A) ? B : A;
    for (int j = threadIdx.x; j < S; j += 256) {
        float a = bf2f(wr[j * DF + k]), b = bf2f(wi[j * DF + k]); float2 t = r[j];
        r[j] = make_float2(t.x * a - t.y * b, t.x * b + t.y * a);
    }
    float2* q = fft_lds<S, 1>(r, o);
    for (int s = threadIdx.x; s < S; s += 256) R[(size_t)s * DF + k] = q[s];
}
__global__ __launch_bounds__(256) void k3(const float2* R, const u16* x, const u16* wrt, u16* out, u16* hbuf, u16* lnbuf, float* rbuf) {
    __shared__ float2 A[D], B[D]; __shared__ float red[4];
    int s = blockIdx.x;
    for (int k = threadIdx.x; k < DF; k += 256) {
        float2 t = R[(size_t)s * DF + k];
        if (k == 0 || k == 512) { A[k] = make_float2(t.x, 0.f); }
        else { A[k] = t; A[D - k] = make_float2(t.x, -t.y); }
    }
    float2* r = fft_lds<D, 1>(A, B);
    float v[4]; float sum = 0;
    for (int i = 0; i < 4; ++i) { int d = threadIdx.x + 256 * i; float y = rbf(r[d].x * (1.0f / (S * D))); v[i] = rbf(bf2f(x[s * D + d]) + y); sum += v[i]; }
    float mean = block_sum(sum, red) / D; float q = 0;
    for (int i = 0; i < 4; ++i) { float d = v[i] - mean; q += d * d; }
    float var = rbf(block_sum(q, red) / D); float m = rbf(mean);
    float sd = rbf(sqrtf(rbf(var + 1e-5f)));
    float dot = 0;
    for (int i = 0; i < 4; ++i) { int d = threadIdx.x + 256 * i; float l = rbf(rbf(v[i] - m) / sd); dot += l * bf2f(wrt[d]);
        out[s * D + d] = f2bf(v[i] + l); hbuf[s * D + d] = f2bf(v[i]); lnbuf[s * D + d] = f2bf(l); }
    dot = block_sum(dot, red);
    if (threadIdx.x == 0) rbuf[s] = rbf(dot);
}
DI void topk(const float* rbuf, int* idx, float* sr) {
    for (int i = threadIdx.x; i < S; i += 256) sr[i] = rbuf[i];
    __syncthreads();
    for (int i = threadIdx.x; i < S; i += 256) { float v = sr[i]; int rank = 0;
        for (int j = 0; j < S; ++j) { float w = sr[j]; rank += (w > v) || (w == v && j < i); }
        if (rank < C) idx[rank] = i; }
    __syncthreads();
}
__global__ __launch_bounds__(256) void k4a(const float* rbuf, int* gidx) {
    __shared__ float sr[S]; __shared__ int idx[C];
    topk(rbuf, idx, sr);
    if (threadIdx.x < C) gidx[threadIdx.x] = idx[threadIdx.x];
}
__global__ __launch_bounds__(256) void k4(const int* gidx, const u16* lnbuf, const u16* wg, const u16* wu, u16* act) {
    __shared__ float xs[C][D];
    for (int i = threadIdx.x; i < C * D; i += 256) xs[i >> 10][i & 1023] = bf2f(lnbuf[gidx[i >> 10] * D + (i & 1023)]);
    __syncthreads();
    int f = blockIdx.x * 256 + threadIdx.x; float g[C], u[C];
    for (int c = 0; c < C; ++c) g[c] = u[c] = 0;
    for (int d = 0; d < D; ++d) { float a = bf2f(wg[d * F + f]), b = bf2f(wu[d * F + f]);
        for (int c = 0; c < C; ++c) { g[c] += xs[c][d] * a; u[c] += xs[c][d] * b; } }
    for (int c = 0; c < C; ++c) { float gg = rbf(g[c]); gg = rbf(gg / (1.f + expf(-gg))); act[c * F + f] = f2bf(gg * rbf(u[c])); }
}
__global__ __launch_bounds__(256) void k5(const int* gidx, const float* rbuf, const u16* act, const u16* wd, const u16* hbuf, const u16* lnbuf, u16* out) {
    int d = blockIdx.x * 256 + threadIdx.x; float y[C];
    for (int c = 0; c < C; ++c) y[c] = 0;
    for (int f = 0; f < F; ++f) { float w = bf2f(wd[f * D + d]); for (int c = 0; c < C; ++c) y[c] += bf2f(act[c * F + f]) * w; }
    for (int c = 0; c < C; ++c) { int s = gidx[c]; float sg = rbf(1.f / (1.f + expf(-rbuf[s]))); float yy = rbf(rbf(y[c]) * sg);
        out[s * D + d] = f2bf(bf2f(hbuf[s * D + d]) + rbf(bf2f(lnbuf[s * D + d]) + yy)); }
}
extern "C" void kernel_launch(void* const* d_in, const int* in_sizes, int n_in, void* d_out, int out_size, void* d_ws, size_t ws_size, hipStream_t stream) {
    const u16* x = (const u16*)d_in[0]; const u16* wr = (const u16*)d_in[1]; const u16* wi = (const u16*)d_in[2];
    const u16* wrt = (const u16*)d_in[4]; const u16* wg = (const u16*)d_in[5]; const u16* wu = (const u16*)d_in[6]; const u16* wd = (const u16*)d_in[7];
    char* ws = (char*)d_ws;
    float2* R = (float2*)ws; u16* hbuf = (u16*)(ws + (16 << 20)); u16* lnbuf = (u16*)(ws + (24 << 20)); float* rbuf = (float*)(ws + (32 << 20));
    int* gidx = (int*)(ws + (33 << 20)); u16* act = (u16*)(ws + (34 << 20));
    k1<<<S, 256, 0, stream>>>(x, R);
    k2<<<DF, 256, 0, stream>>>(R, wr, wi);
    k3<<<S, 256, 0, stream>>>(R, x, wrt, (u16*)d_out, hbuf, lnbuf, rbuf);
    k4a<<<1, 256, 0, stream>>>(rbuf, gidx);
    k4<<<F / 256, 256, 0, stream>>>(gidx, lnbuf, wg, wu, act);
    k5<<<D / 256, 256, 0, stream>>>(gidx, rbuf, act, wd, hbuf, lnbuf, (u16*)d_out);
}
```

```cpp
#include <hip/hip_runtime.h>
#include <stdint.h>

typedef unsigned short u16;
typedef unsigned u32;
typedef unsigned long long u64;
typedef u32 v4u __attribute__((ext_vector_type(4)));
typedef u32 v2u __attribute__((ext_vector_type(2)));
typedef float f2 __attribute__((ext_vector_type(2)));
typedef float f4 __attribute__((ext_vector_type(4)));
typedef float f16v __attribute__((ext_vector_type(16)));
typedef _Float16 h2 __attribute__((ext_vector_type(2)));
typedef _Float16 h4 __attribute__((ext_vector_type(4)));
typedef _Float16 h8 __attribute__((ext_vector_type(8)));
typedef __bf16 b2 __attribute__((ext_vector_type(2)));
typedef __bf16 b8 __attribute__((ext_vector_type(8)));
typedef short s4 __attribute__((ext_vector_type(4)));
#define DI __device__ __forceinline__
#define GAS __attribute__((address_space(1)))
#define LAS __attribute__((address_space(3)))
#define BC(T, x) __builtin_bit_cast(T, x)

namespace {
constexpr int S_ = 2048, D_ = 1024, DF_ = 513, F_ = 3584;
constexpr size_t WS_EPOCH = 0;
constexpr size_t WS_FLAG = 4096;
constexpr size_t FLAG_SYNC = 32 * 16 * 256;
constexpr size_t WS_FLAG2 = WS_FLAG + 3 * FLAG_SYNC;
constexpr size_t WS_RK = WS_FLAG2 + 8 * 4 * 256;
constexpr size_t WS_ST = WS_RK + 4096;
constexpr size_t WS_ACT = WS_ST + 1024 * 64;
constexpr size_t WS_ACC = WS_ACT + 8 * 2 * 7 * 1024;
constexpr size_t WS_E = 1 << 20;
constexpr size_t WS_H = WS_E + (4 << 20);
constexpr size_t WS_TS = WS_H + (4 << 20);
constexpr int L_W = 0;
constexpr int L_R1 = 131072 - 2 * 17408;
constexpr int L_WF = 131072 - 17408;
constexpr int L_R0 = 131072;
constexpr int L_SM = L_R0 + 17408;
constexpr int L_CAND = L_SM + 256;
constexpr int L_TOTAL = L_CAND + 8192;
constexpr u32 SPIN_CAP = 1u << 20;

DI float u2f(u32 x) { return BC(float, x); }
DI u32 f2u(float x) { return BC(u32, x); }
DI u32 pkh(float a, float b) { f2 v = {a, b}; return BC(u32, __builtin_convertvector(v, h2)); }
DI u32 pkb(float a, float b) { f2 v = {a, b}; return BC(u32, __builtin_convertvector(v, b2)); }
DI float rbf(float a) { return u2f(pkb(a, a) << 16); }
DI float blo(u32 d) { return u2f(d << 16); }
DI float bhi(u32 d) { return u2f(d & 0xffff0000u); }
DI float dot2(u32 a, u32 b, float c) { return __builtin_amdgcn_fdot2_f32_bf16(BC(b2, a), BC(b2, b), c, false); }
DI f16v mm32h(v4u a, v4u b, f16v c) { return __builtin_amdgcn_mfma_f32_32x32x16_f16(BC(h8, a), BC(h8, b), c, 0, 0, 0); }
DI f16v mm32b(v4u a, v4u b, f16v c) { return __builtin_amdgcn_mfma_f32_32x32x16_bf16(BC(b8, a), BC(b8, b), c, 0, 0, 0); }
DI f4 mm16h(v4u a, v4u b, f4 c) { return __builtin_amdgcn_mfma_f32_16x16x32_f16(BC(h8, a), BC(h8, b), c, 0, 0, 0); }
DI f4 mm16b(v4u a, v4u b, f4 c) { return __builtin_amdgcn_mfma_f32_16x16x32_bf16(BC(b8, a), BC(b8, b), c, 0, 0, 0); }
DI f4 mm16k16(v2u a, v2u b, f4 c) { return __builtin_amdgcn_mfma_f32_16x16x16f16(BC(h4, a), BC(h4, b), c, 0, 0, 0); }
DI void bar() { asm volatile("s_waitcnt lgkmcnt(0)\n\ts_barrier" ::: "memory"); }
DI void drain() { asm volatile("s_waitcnt vmcnt(0)" ::: "memory"); }
DI float sinr(float r) { return __builtin_amdgcn_sinf(r); }
DI float cosr(float r) { return __builtin_amdgcn_cosf(r); }
template <int CTRL> DI float dppf(float v) { return u2f((u32)__builtin_amdgcn_update_dpp(0, (int)f2u(v), CTRL, 0xf, 0xf, true)); }
template <int CTRL> DI u32 dppu(u32 v) { return (u32)__builtin_amdgcn_update_dpp(0, (int)v, CTRL, 0xf, 0xf, true); }
DI float rowsum(float v) { v += dppf<0x128>(v); v += dppf<0x124>(v); v += dppf<0x122>(v); v += dppf<0x121>(v); return v; }
DI float swapadd32(float a, float b) { auto r = __builtin_amdgcn_permlane32_swap(f2u(a), f2u(b), false, false); const u32 x = r[0], y = r[1]; return u2f(x) + u2f(y); }
DI float swapadd16(float a, float b) { auto r = __builtin_amdgcn_permlane16_swap(f2u(a), f2u(b), false, false); const u32 x = r[0], y = r[1]; return u2f(x) + u2f(y); }
DI float rdl(float v, int lane) { return u2f((u32)__builtin_amdgcn_readlane((int)f2u(v), lane)); }
DI float red4(float a, float b, float c, float d) { return rowsum(swapadd16(swapadd32(a, b), swapadd32(c, d))); }
#define RSRC(p, n) __builtin_amdgcn_make_buffer_rsrc((void*)(p), 0, (n), 0x00020000)
constexpr int AUX_NT = 2, AUX_SC1 = 16;

#define STAMP(i) do {} while (0)

DI void grid_sync(char* base, int p, int rep, u32 tag, int l) {
    if (l < 32) { GAS u32* a = (GAS u32*)(base + l * 4096 + (p >> 4) * 256 + (p & 15) * 4); asm volatile("global_atomic_swap %0, %1, off sc1" :: "v"(a), "v"(tag) : "memory"); }
    GAS u64* p0 = (GAS u64*)(base + rep * 4096 + (l >> 3) * 256 + (l & 7) * 8);
    const u64 want = ((u64)tag << 32) | tag, zero = 0;
    bool ok0 = false, ok1 = false;
    for (u32 sp = 0; sp < SPIN_CAP; ++sp) {
        u64 v0 = want, v1 = want;
        if (!ok0) asm volatile("global_atomic_add_x2 %0, %1, %2, off sc0 sc1" : "=&v"(v0) : "v"(p0), "v"(zero) : "memory");
        if (!ok1) asm volatile("global_atomic_add_x2 %0, %1, %2, off offset:2048 sc0 sc1" : "=&v"(v1) : "v"(p0), "v"(zero) : "memory");
        asm volatile("s_waitcnt vmcnt(0)" : "+v"(v0), "+v"(v1) :: "memory");
        ok0 = v0 == want; ok1 = v1 == want;
        if (__all(ok0 && ok1)) break;
    }
}

__global__ void __launch_bounds__(384) fused(const u16* __restrict__ x, const u16* __restrict__ fwr, const u16* __restrict__ fwi, const u16* __restrict__ wrt,
                                             const u16* __restrict__ wg, const u16* __restrict__ wu, const u16* __restrict__ wd, u16* __restrict__ out, char* __restrict__ ws) {
    extern __shared__ __attribute__((aligned(16))) char smem[];
    const int p = (blockIdx.x & 7) * 32 + (blockIdx.x >> 3);
    const int t = threadIdx.x, w = __builtin_amdgcn_readfirstlane(t >> 6), l = t & 63;
    const int grp = p >> 5, rank = p & 31;
    const bool roleG = rank < 14, roleD = rank >= 14 && rank < 30;
    LAS char* sm = (LAS char*)smem;
    STAMP(0);

    if (w == 4) {
        const u16* wsrc; int rstride;
        if (roleG) { wsrc = ((rank & 1) ? wu : wg) + grp * 448 + (rank >> 1) * 64; rstride = F_; }
        else { wsrc = wd + (size_t)grp * 448 * D_ + (roleD ? (rank - 14) * 64 : 0); rstride = D_; }
        wsrc += (l >> 3) * rstride + (l & 7) * 8;
        const int nfill = roleG ? 128 : (roleD ? 56 : 0);
        int f = 0;
        auto fill = [&](int upto) { if (upto > nfill) upto = nfill;
            for (; f < upto; ++f) __builtin_amdgcn_global_load_lds((const GAS void*)(wsrc + (size_t)f * 8 * rstride), (LAS void*)(sm + L_W + f * 1024), 16, 0, AUX_NT); };
        __builtin_amdgcn_s_sleep(2);
#pragma unroll
        for (int i = 0; i < 16; ++i) { const int j = grp + 8 * ((i >> 1) * 32 + (l >> 1));
            __builtin_amdgcn_global_load_lds((const GAS void*)(((i & 1) ? fwi : fwr) + j * DF_ + 16 * rank + (l & 1) * 8), (LAS void*)(sm + L_WF + i * 1024), 16, 0, AUX_NT); }
        if (rank == 0) {
#pragma unroll
            for (int i = 0; i < 8; ++i) { const int j = grp + 8 * ((i >> 1) * 64 + l);
                __builtin_amdgcn_global_load_lds((const GAS void*)(((i & 1) ? fwi : fwr) + j * DF_ + 511), (LAS void*)(sm + L_CAND + i * 256), 4, 0, AUX_NT); }
        }
        fill(16);
        bar();
        drain();
        bar();
        bar();
        __builtin_amdgcn_s_sleep(2);
        fill(32);
        bar();
        fill(44);
        bar();
        fill(60);
        bar();
        bar();
        __builtin_amdgcn_s_sleep(2);
        fill(84);
        bar();
        drain();
        fill(128);
        drain();
        bar();
        return;
    }
    if (w == 5) {
        const u32 tag = *(const u32*)(ws + WS_EPOCH) + 1u;
        const int rep = blockIdx.x & 31;
        { auto ra = RSRC(ws + WS_ACC, 65536); __builtin_amdgcn_raw_buffer_store_b32(0u, ra, p * 256 + l * 4, 0, AUX_SC1); }
        if (l == 0) *(LAS u32*)(sm + L_SM) = 0u;
        if (l == 0) *(LAS u32*)(sm + L_SM + 4) = tag;
        drain();
        bar(); bar();
        grid_sync(ws + WS_FLAG, p, rep, tag, l);
        bar();
        if (blockIdx.x == 0 && l == 0) *(u32*)(ws + WS_EPOCH) = tag;
        bar(); bar(); bar();
        grid_sync(ws + WS_FLAG + FLAG_SYNC, p, rep, tag, l);
        bar();
        bar(); bar();
        grid_sync(ws + WS_FLAG + 2 * FLAG_SYNC, p, rep, tag, l);
        bar();
        if (roleD) {
            bar(); bar(); bar();
            GAS u32* pp = (GAS u32*)(ws + WS_FLAG2 + (grp * 4 + (rank & 3)) * 256 + l * 4);
            bool ok = l >= 56;
            for (u32 sp = 0; sp < SPIN_CAP; ++sp) {
                u32 v = tag;
                if (!ok) asm volatile("global_atomic_add %0, %1, %2, off sc0 sc1" : "=&v"(v) : "v"(pp), "v"(0u) : "memory");
                asm volatile("s_waitcnt vmcnt(0)" : "+v"(v) :: "memory");
                ok = v == tag;
                if (__all(ok)) break;
            }
            bar();
        }
        return;
    }

    const int lam = l & 31, hh = l >> 5;
    const int pl = (lam & 0x13) | ((lam & 4) << 1) | ((lam & 8) >> 1);
    const int l15 = l & 15, g4 = l >> 4;
    auto rx = RSRC(x, S_ * D_ * 2);
    v4u xr[2][2];
    { const int vo = (p + 256 * w) * 2048 + 64 * pl + 16 * hh;
      xr[0][0] = __builtin_amdgcn_raw_buffer_load_b128(rx, vo, 0, AUX_NT);
      xr[1][0] = __builtin_amdgcn_raw_buffer_load_b128(rx, vo, 1024 * 2048, AUX_NT);
      xr[0][1] = __builtin_amdgcn_raw_buffer_load_b128(rx, vo + 32, 0, AUX_NT);
      xr[1][1] = __builtin_amdgcn_raw_buffer_load_b128(rx, vo + 32, 1024 * 2048, AUX_NT); }
    v4u wq[2];
    { auto rw = RSRC(wrt, 2048); wq[0] = __builtin_amdgcn_raw_buffer_load_b128(rw, 64 * pl + 16 * hh, 0, 0); wq[1] = __builtin_amdgcn_raw_buffer_load_b128(rw, 64 * pl + 16 * hh + 32, 0, 0); }

    v4u M1[4], M2[4]; f16v twc, tws;
#pragma unroll
    for (int r = 0; r < 16; ++r) {
        const int th = pl * (16 * (r >> 3) + (r & 7)) + pl * 8 * hh;
        const float a32 = (float)(th & 31) * (1.f / 32.f), a1k = (float)th * (1.f / 1024.f);
        const float c = cosr(a32), s = sinr(a32);
        M1[r >> 2][r & 3] = pkh(c, s); M2[r >> 2][r & 3] = pkh(-s, c);
        twc[r] = cosr(a1k) * (1.f / 32.f); tws[r] = sinr(a1k) * (1.f / 32.f);
    }
    v4u PI[2];
#pragma unroll
    for (int s2 = 0; s2 < 2; ++s2)
#pragma unroll
        for (int d = 0; d < 4; ++d) { const int k = pl - 16 * s2 - 8 * hh - 2 * d; PI[s2][d] = k == 0 ? 0x3f80u : (k == 1 ? 0x3f800000u : 0u); }
    v2u D8f, D8i;
    { const int q = l15 >> 1; const float e = (q & 1) ? -1.f : 1.f, a = (float)((q * g4) & 7) * 0.125f, c = cosr(a) * 0.25f, s = sinr(a) * 0.25f;
      if (l15 & 1) { D8f[0] = pkh(-e * c - s, c - e * s); D8f[1] = pkh(e * c - s, -c - e * s); }
      else         { D8f[0] = pkh(c - e * s, e * c + s);  D8f[1] = pkh(c + e * s, e * c - s); } }
#pragma unroll
    for (int x2 = 0; x2 < 2; ++x2) { const int q = 2 * g4 + x2, i = l15 >> 2, j = l15 & 3; const float e = x2 ? -1.f : 1.f, a = (float)((q * i) & 7) * 0.125f, c = cosr(a) * 0.25f, s = sinr(a) * 0.25f;
      const float cr = j == 0 ? c - e * s : j == 1 ? s + e * c : j == 2 ? c + e * s : e * c - s;
      const float ci = j == 0 ? -s - e * c : j == 1 ? c - e * s : j == 2 ? e * c - s : -c - e * s;
      D8i[x2] = pkh(cr, ci); }
    f16v zero16; for (int i = 0; i < 16; ++i) zero16[i] = 0.f;
    const f4 zero4 = {0.f, 0.f, 0.f, 0.f};
    STAMP(1);

    auto rE = RSRC(ws + WS_E, 4 << 20);
    const int eoff = p * 16384 + g4 * 4096 + (128 * w + 4 * l15) * 4;
    {
        f16v xa = mm32b(xr[0][0], PI[0], zero16); xa = mm32b(xr[0][1], PI[1], xa);
        f16v xb = mm32b(xr[1][0], PI[0], zero16); xb = mm32b(xr[1][1], PI[1], xb);
        float s1a = 0.f, s2a = 0.f, s1b = 0.f, s2b = 0.f;
#pragma unroll
        for (int i = 0; i < 8; ++i) { const u32 a = xr[0][i >> 2][i & 3], b = xr[1][i >> 2][i & 3];
            s1a = dot2(a, 0x3f803f80u, s1a); s2a = dot2(a, a, s2a); s1b = dot2(b, 0x3f803f80u, s1b); s2b = dot2(b, b, s2b); }
        STAMP(2);
        const float red = red4(s1a, s1b, s2a, s2b);
        const float ma = rdl(red, 0) * (1.f / 1024.f), mb = rdl(red, 32) * (1.f / 1024.f);
        const float va = fmaxf(rdl(red, 16) * (1.f / 1024.f) - ma * ma, 0.f), vb = fmaxf(rdl(red, 48) * (1.f / 1024.f) - mb * mb, 0.f);
        const float ra = 1.f / rbf(__builtin_sqrtf(rbf(rbf(va) + 1e-5f))), rb = 1.f / rbf(__builtin_sqrtf(rbf(rbf(vb) + 1e-5f)));
        const float oa = -ma * ra, ob = -mb * rb;
        v4u z[4];
#pragma unroll
        for (int r = 0; r < 16; ++r) z[r >> 2][r & 3] = pkh(xa[r] * ra + oa, xb[r] * rb + ob);
        f16v tr = zero16, ti = zero16;
#pragma unroll
        for (int s = 0; s < 4; ++s) { tr = mm32h(z[s], M1[s], tr); ti = mm32h(z[s], M2[s], ti); }
        const f16v ur = tr * twc + ti * tws, ui = ti * twc - tr * tws;
#pragma unroll
        for (int r = 0; r < 16; ++r) z[r >> 2][r & 3] = pkh(ur[r], ui[r]);
        tr = zero16; ti = zero16;
#pragma unroll
        for (int s = 0; s < 4; ++s) { tr = mm32h(M1[s], z[s], tr); ti = mm32h(M2[s], z[s], ti); }
        u32 zz[16];
#pragma unroll
        for (int r = 0; r < 16; ++r) zz[r] = pkh(tr[r], ti[r]);
        if (l == 0) zz[0] = zz[8];
        LAS char* zb = sm + L_R0 + w * 4112;
        const int lo = (pl + 256 * hh) * 8, hi = (512 - pl - 256 * hh) * 8 + 4;
#pragma unroll
        for (int r = 0; r < 8; ++r) *(LAS u32*)(zb + lo + 256 * r) = zz[r];
        *(LAS u32*)(zb + (hi & 4095)) = zz[8];
#pragma unroll
        for (int r = 1; r < 8; ++r) *(LAS u32*)(zb + hi - 256 * r) = zz[8 + r];
        bar();
        STAMP(3);
        LAS char* zr = sm + L_R0 + g4 * 4112 + (128 * w + 4 * l15) * 8;
#pragma unroll
        for (int u = 0; u < 2; ++u) {
            const v4u q0 = *(LAS v4u*)(zr + u * 512), q1 = *(LAS v4u*)(zr + u * 512 + 16);
            const f4 d0 = mm16k16(D8f, v2u{q0[0], q0[1]}, zero4), d1 = mm16k16(D8f, v2u{q0[2], q0[3]}, zero4);
            const f4 d2 = mm16k16(D8f, v2u{q1[0], q1[1]}, zero4), d3 = mm16k16(D8f, v2u{q1[2], q1[3]}, zero4);
            const v4u o0 = {pkh(d0[0], d0[1]), pkh(d1[0], d1[1]), pkh(d2[0], d2[1]), pkh(d3[0], d3[1])};
            const v4u o1 = {pkh(d0[2], d0[3]), pkh(d1[2], d1[3]), pkh(d2[2], d2[3]), pkh(d3[2], d3[3])};
            __builtin_amdgcn_raw_buffer_store_b128(o0, rE, eoff + u * 256, 0, AUX_SC1);
            __builtin_amdgcn_raw_buffer_store_b128(o1, rE, eoff + u * 256 + 2048, 0, AUX_SC1);
        }
        STAMP(4);
    }
    drain();
    bar();
    STAMP(5);

    {
        v4u A1[2], A4[2], A2[4][2], A3[4][2];
#pragma unroll
        for (int i = 0; i < 4; ++i) {
            { const float a = (float)(((4 * g4 + i) * (8 * l15 + grp)) & 127) * (1.f / 128.f), c = cosr(a) * 0.25f, s = sinr(a) * 0.25f; A1[0][i] = pkh(c, s); A1[1][i] = pkh(-s, c); }
            { const float a = (float)((l15 * (8 * (4 * g4 + i) + grp)) & 127) * (1.f / 128.f), c = cosr(a) * 0.25f, s = sinr(a) * 0.25f; A4[0][i] = pkh(c, -s); A4[1][i] = pkh(s, c); }
#pragma unroll
            for (int tau = 0; tau < 4; ++tau) { const int j0 = 8 * (4 * w + tau) + grp;
                { const float a = (float)(((4 * g4 + i) * (j0 + 128 * l15)) & 2047) * (1.f / 2048.f), c = cosr(a) * 0.25f, s = sinr(a) * 0.25f; A2[tau][0][i] = pkh(c, s); A2[tau][1][i] = pkh(-s, c); }
                { const float a = (float)((l15 * (j0 + 128 * (4 * g4 + i))) & 2047) * (1.f / 2048.f), c = cosr(a) * 0.25f, s = sinr(a) * 0.25f; A3[tau][0][i] = pkh(c, -s); A3[tau][1][i] = pkh(s, c); } }
        }
        f4 fr[4], fi[4];
        { const bool ny = rank == 0 && l15 == 0;
#pragma unroll
          for (int tau = 0; tau < 4; ++tau)
#pragma unroll
            for (int i = 0; i < 4; ++i) { const int m = 4 * w + tau + 16 * (4 * g4 + i);
                const int a0 = ny ? L_CAND + (m >> 6) * 512 + (m & 63) * 4 + 2 : L_WF + (m >> 5) * 2048 + (m & 31) * 32 + l15 * 2;
                fr[tau][i] = u2f((u32)*(LAS u16*)(sm + a0) << 16); fi[tau][i] = u2f((u32)*(LAS u16*)(sm + a0 + (ny ? 256 : 1024)) << 16); } }
        bar();
        STAMP(6);
        const int goff = (64 * g4 + 4 * w + ((l >> 2) & 3)) * 16384 + p * 64 + (l & 3) * 16;
        v4u v[4];
#pragma unroll
        for (int i = 0; i < 4; ++i) v[i] = __builtin_amdgcn_raw_buffer_load_b128(rE, goff, i * 16 * 16384, AUX_SC1);
        { LAS char* b0 = sm + L_R1 + l15 * 1040 + g4 * 4 + w * 16; LAS char* b1 = sm + L_R1 + l15 * 1040 + g4 * 4 + (w ^ 2) * 16;
#pragma unroll
          for (int e = 0; e < 4; ++e) { const v4u f = {v[0][e], v[1][e], v[2][e], v[3][e]};
            const f4 tr = mm16h(f, A1[0], zero4), ti = mm16h(f, A1[1], zero4);
#pragma unroll
            for (int i = 0; i < 4; ++i) *(LAS u32*)((i < 2 ? b0 : b1) + (4 * i + e) * 64) = pkh(tr[i], ti[i]); } }
        STAMP(7);
        bar();
        v4u uu[4];
        { LAS char* b = sm + L_R1 + 4 * w * 1040 + l15 * 64 + (g4 ^ ((l15 >> 3) << 1)) * 16;
#pragma unroll
          for (int tau = 0; tau < 4; ++tau) { const v4u f = *(LAS v4u*)(b + tau * 1040);
            const f4 xr_ = mm16h(A2[tau][0], f, zero4), xi_ = mm16h(A2[tau][1], f, zero4);
            const f4 yr = xr_ * fr[tau] - xi_ * fi[tau], yi = xr_ * fi[tau] + xi_ * fr[tau];
            const v4u y = {pkh(yr[0], yi[0]), pkh(yr[1], yi[1]), pkh(yr[2], yi[2]), pkh(yr[3], yi[3])};
            const f4 ur = mm16h(A3[tau][0], y, zero4), ui = mm16h(A3[tau][1], y, zero4);
#pragma unroll
            for (int i = 0; i < 4; ++i) uu[i][tau] = pkh(ur[i], ui[i]); } }
        { LAS char* b = sm + L_R0 + 4 * g4 * 1088 + l15 * 64 + (w ^ ((l15 >> 1) & 3) ^ (l15 >> 3)) * 16;
#pragma unroll
          for (int i = 0; i < 4; ++i) *(LAS v4u*)(b + i * 1088) = uu[i]; }
        bar();
        STAMP(8);
        { const int tq = l & 3; LAS char* b = sm + L_R0 + (4 * w + ((l >> 2) & 3)) * 1088 + tq * 256;
#pragma unroll
          for (int e = 0; e < 4; ++e) { const int col = 4 * tq + e; const v4u f = *(LAS v4u*)(b + e * 64 + (g4 ^ ((col >> 1) & 3) ^ (col >> 3)) * 16);
            const f4 orr = mm16h(A4[0], f, zero4), oi = mm16h(A4[1], f, zero4);
#pragma unroll
            for (int i = 0; i < 4; ++i) v[i][e] = pkh(orr[i], oi[i]); } }
#pragma unroll
        for (int i = 0; i < 4; ++i) __builtin_amdgcn_raw_buffer_store_b128(v[i], rE, goff, i * 16 * 16384, AUX_SC1);
        STAMP(9);
    }
    drain();
    bar();
    STAMP(10);

    f16v ha, hb;
#pragma unroll
    for (int r = 0; r < 16; r += 2) { const u32 a = xr[0][r >> 3][(r >> 1) & 3], b = xr[1][r >> 3][(r >> 1) & 3]; ha[r] = blo(a); ha[r + 1] = bhi(a); hb[r] = blo(b); hb[r + 1] = bhi(b); }
    float wsum;
    { float s = 0.f;
#pragma unroll
      for (int i = 0; i < 8; ++i) s = dot2(wq[i >> 2][i & 3], 0x3f803f80u, s);
      wsum = rdl(red4(s, s, s, s), 0); }
    bar();
    STAMP(11);
    v4u hA[2], hB[2];
    {
        v4u v[2][2];
#pragma unroll
        for (int u = 0; u < 2; ++u) { v[u][0] = __builtin_amdgcn_raw_buffer_load_b128(rE, eoff + u * 256, 0, AUX_SC1); v[u][1] = __builtin_amdgcn_raw_buffer_load_b128(rE, eoff + u * 256 + 2048, 0, AUX_SC1); }
        LAS char* zw = sm + L_R0 + g4 * 4096 + (128 * w + 4 * l15) * 4;
#pragma unroll
        for (int u = 0; u < 2; ++u) { v4u P, Q;
#pragma unroll
            for (int e = 0; e < 4; ++e) { const f4 d = mm16k16(D8i, v2u{v[u][0][e], v[u][1][e]}, zero4); P[e] = pkh(d[1], d[0]); Q[e] = pkh(d[3], d[2]); }
            *(LAS v4u*)(zw + u * 256) = P; *(LAS v4u*)(zw + u * 256 + 2048) = Q; }
        STAMP(12);
        bar();
        LAS char* zb = sm + L_R0 + w * 4096;
        const int lo = (pl + 256 * hh) * 4, hi = (512 - pl - 256 * hh) * 4;
        v4u z[4];
#pragma unroll
        for (int r = 0; r < 8; ++r) z[r >> 2][r & 3] = *(LAS u32*)(zb + lo + 128 * r);
        z[2][0] = *(LAS u32*)(zb + 2048 + (hi & 2047));
#pragma unroll
        for (int r = 1; r < 8; ++r) z[2 + (r >> 2)][r & 3] = *(LAS u32*)(zb + 2048 + hi - 128 * r);
        { const u32 z0 = z[0][0], z8 = z[2][0]; const h2 hf = {(_Float16)0.5f, (_Float16)0.5f}; const h2 av = BC(h2, z0) * hf + BC(h2, z8) * hf;
          z[0][0] = l == 0 ? 0u : z0; z[2][0] = l == 0 ? BC(u32, av) : z8; }
        f16v tr = zero16, ti = zero16;
#pragma unroll
        for (int s = 0; s < 4; ++s) { tr = mm32h(z[s], M1[s], tr); ti = mm32h(z[s], M2[s], ti); }
        const f16v ur = tr * twc + ti * tws, ui = ti * twc - tr * tws;
#pragma unroll
        for (int r = 0; r < 16; ++r) z[r >> 2][r & 3] = pkh(ur[r], ui[r]);
#pragma unroll
        for (int s = 0; s < 4; ++s) { hb = mm32h(z[s], M1[s], hb); ha = mm32h(z[s], M2[s], ha); }
#pragma unroll
        for (int r = 0; r < 16; r += 2) { hA[r >> 3][(r >> 1) & 3] = pkb(ha[r], ha[r + 1]); hB[r >> 3][(r >> 1) & 3] = pkb(hb[r], hb[r + 1]); }
    }
    auto rH = RSRC(ws + WS_H, 4 << 20);
    { const int ho = (p * 8 + 2 * w) * 2048 + l * 16;
      __builtin_amdgcn_raw_buffer_store_b128(hA[0], rH, ho, 0, AUX_SC1); __builtin_amdgcn_raw_buffer_store_b128(hA[1], rH, ho + 1024, 0, AUX_SC1);
      __builtin_amdgcn_raw_buffer_store_b128(hB[0], rH, ho + 2048, 0, AUX_SC1); __builtin_amdgcn_raw_buffer_store_b128(hB[1], rH, ho + 3072, 0, AUX_SC1); }
    STAMP(13);
    float mA, mB, rsA, rsB;
    {
        float s1a = 0.f, s2a = 0.f, s3a = 0.f, s1b = 0.f, s2b = 0.f, s3b = 0.f;
#pragma unroll
        for (int i = 0; i < 8; ++i) { const u32 a = hA[i >> 2][i & 3], b = hB[i >> 2][i & 3], q = wq[i >> 2][i & 3];
            s1a = dot2(a, 0x3f803f80u, s1a); s2a = dot2(a, a, s2a); s3a = dot2(a, q, s3a); s1b = dot2(b, 0x3f803f80u, s1b); s2b = dot2(b, b, s2b); s3b = dot2(b, q, s3b); }
        const float red = red4(s1a, s1b, s2a, s2b), red3 = red4(s3a, s3b, s3a, s3b);
        const float ma = rdl(red, 0) * (1.f / 1024.f), mb = rdl(red, 32) * (1.f / 1024.f);
        const float va = fmaxf(rdl(red, 16) * (1.f / 1024.f) - ma * ma, 0.f), vb = fmaxf(rdl(red, 48) * (1.f / 1024.f) - mb * mb, 0.f);
        const float sa = rbf(__builtin_sqrtf(rbf(rbf(va) + 1e-5f))), sb = rbf(__builtin_sqrtf(rbf(rbf(vb) + 1e-5f)));
        mA = rbf(ma); mB = rbf(mb); rsA = 1.f / sa; rsB = 1.f / sb;
        const u32 rr = pkb((rdl(red3, 0) - mA * wsum) * rsA, (rdl(red3, 32) - mB * wsum) * rsB);
        if (l == 0) { GAS u32* a = (GAS u32*)(ws + WS_RK + (p * 4 + w) * 4); asm volatile("global_atomic_swap %0, %1, off sc1" :: "v"(a), "v"(rr) : "memory");
            auto rs = RSRC(ws + WS_ST, 65536); const v2u st = {(f2u(mA) >> 16) | (f2u(sa) & 0xffff0000u), (f2u(mB) >> 16) | (f2u(sb) & 0xffff0000u)};
            __builtin_amdgcn_raw_buffer_store_b64(st, rs, (p * 4 + w) * 64, 0, AUX_SC1); }
    }
    STAMP(14);
    drain();
    bar();
    STAMP(15);
    v4u oA[2], oB[2];
    {
        LAS char* ob = sm + L_R0 + w * 4352;
#pragma unroll
        for (int s2 = 0; s2 < 2; ++s2) { v4u a, b;
#pragma unroll
            for (int d = 0; d < 4; ++d) {
                { const float h0 = blo(hA[s2][d]), h1 = bhi(hA[s2][d]); const u32 c = pkb(h0 - mA, h1 - mA), n = pkb(blo(c) * rsA, bhi(c) * rsA); a[d] = pkb(h0 + blo(n), h1 + bhi(n)); }
                { const float h0 = blo(hB[s2][d]), h1 = bhi(hB[s2][d]); const u32 c = pkb(h0 - mB, h1 - mB), n = pkb(blo(c) * rsB, bhi(c) * rsB); b[d] = pkb(h0 + blo(n), h1 + bhi(n)); } }
            const int o = 64 * pl + (((2 * s2 + hh) ^ (pl >> 1)) & 3) * 16;
            *(LAS v4u*)(ob + o) = a; *(LAS v4u*)(ob + 2048 + o) = b; }
        asm volatile("s_waitcnt lgkmcnt(0)" ::: "memory");
#pragma unroll
        for (int s2 = 0; s2 < 2; ++s2) { const int a_ = 16 * s2 + (l >> 2), o = 64 * a_ + (((l & 3) ^ (a_ >> 1)) & 3) * 16; oA[s2] = *(LAS v4u*)(ob + o); oB[s2] = *(LAS v4u*)(ob + 2048 + o); }
    }
    v4u Bf[8][4];
    if (roleG) {
        const int s2 = w >> 1, h_ = w & 1;
#pragma unroll
        for (int k = 0; k < 8; ++k) { const int la = 4 * k + g4, pa = (la & 0x13) | ((la & 4) << 1) | ((la & 8) >> 1);
            LAS char* b = sm + L_W + (32 * pa + 16 * s2 + 8 * h_ + (l15 >> 2)) * 128 + (l15 & 3) * 8;
#pragma unroll
            for (int ct = 0; ct < 4; ++ct) { const v2u lo = BC(v2u, __builtin_amdgcn_ds_read_tr16_b64_v4i16((LAS s4*)(b + ct * 32))), hi = BC(v2u, __builtin_amdgcn_ds_read_tr16_b64_v4i16((LAS s4*)(b + ct * 32 + 512)));
                Bf[k][ct] = v4u{lo[0], lo[1], hi[0], hi[1]}; } }
    } else {
#pragma unroll
        for (int k = 0; k < 4; ++k) { const int ks = (w + 4 * k) < 14 ? w + 4 * k : 0;
            LAS char* b = sm + L_W + (32 * ks + 8 * g4 + (l15 >> 2)) * 128 + (l15 & 3) * 8;
#pragma unroll
            for (int ct = 0; ct < 4; ++ct) { const v2u lo = BC(v2u, __builtin_amdgcn_ds_read_tr16_b64_v4i16((LAS s4*)(b + ct * 32))), hi = BC(v2u, __builtin_amdgcn_ds_read_tr16_b64_v4i16((LAS s4*)(b + ct * 32 + 512)));
                Bf[k][ct] = v4u{lo[0], lo[1], hi[0], hi[1]}; } }
    }
    bar();
    STAMP(16);

    LAS u32* smw = (LAS u32*)(sm + L_SM);
    LAS u32* cand = (LAS u32*)(sm + L_CAND);
    {
        auto rk = RSRC(ws + WS_RK, 4096);
        const v4u kv = __builtin_amdgcn_raw_buffer_load_b128(rk, t * 16, 0, AUX_SC1);
        u32 key[8];
#pragma unroll
        for (int j = 0; j < 8; ++j) { const u32 b = (j & 1) ? (kv[j >> 1] & 0xffff0000u) : (kv[j >> 1] << 16);
            key[j] = ((b ^ ((u32)((int)b >> 31) | 0x80000000u)) & 0xffff0000u) | (u32)(2047 - (t * 8 + j)); }
        u32 m = key[0];
#pragma unroll
        for (int j = 1; j < 8; ++j) m = max(m, key[j]);
        m = max(m, dppu<0x128>(m)); m = max(m, dppu<0x124>(m)); m = max(m, dppu<0x122>(m)); m = max(m, dppu<0x121>(m));
        if (l15 == 0) smw[16 + w * 4 + g4] = m;
        bar();
        const v4u a = *(LAS v4u*)(smw + 16), b = *(LAS v4u*)(smw + 20), c = *(LAS v4u*)(smw + 24), d = *(LAS v4u*)(smw + 28);
        const u32 th = min(min(min(max(a[0], a[1]), max(a[2], a[3])), min(max(b[0], b[1]), max(b[2], b[3]))), min(min(max(c[0], c[1]), max(c[2], c[3])), min(max(d[0], d[1]), max(d[2], d[3]))));
#pragma unroll
        for (int j = 0; j < 8; ++j) if (key[j] >= th) { const u32 pos = __hip_atomic_fetch_add(smw, 1u, __ATOMIC_RELAXED, __HIP_MEMORY_SCOPE_WORKGROUP); cand[pos] = key[j]; }
        bar();
        const int n = (int)smw[0];
        for (int i = t; i < n; i += 256) { const u32 k = cand[i]; int rk_ = 0; for (int j = 0; j < n; ++j) rk_ += cand[j] > k; if (rk_ < 8) smw[8 + rk_] = k; }
        bar();
    }
    STAMP(17);
    auto rS = RSRC(ws + WS_ST, 65536);
    auto rA = RSRC(ws + WS_ACT, 8 * 2 * 7 * 1024);
    const u32 tag = smw[1];
    u32 selA = 0, selB = 0;
    {
        const u32 k8 = smw[8 + (l & 7)]; const int pos = 2047 - (int)(k8 & 2047u);
        selA = __any(pos == p * 8 + 2 * w); selB = __any(pos == p * 8 + 2 * w + 1);
    }
    auto store_out = [&]() { auto ro = RSRC(out, S_ * D_ * 2); const int oo = (p + 256 * w) * 2048 + l * 16;
        if (!selA) { __builtin_amdgcn_raw_buffer_store_b128(oA[0], ro, oo, 0, AUX_SC1); __builtin_amdgcn_raw_buffer_store_b128(oA[1], ro, oo + 1024, 0, AUX_SC1); }
        if (!selB) { __builtin_amdgcn_raw_buffer_store_b128(oB[0], ro, oo, 1024 * 2048, AUX_SC1); __builtin_amdgcn_raw_buffer_store_b128(oB[1], ro, oo + 1024, 1024 * 2048, AUX_SC1); } };
    LAS float* part = (LAS float*)(sm + L_CAND);

    if (roleG) {
        const int pos = 2047 - (int)(smw[8 + (l & 7)] & 2047u);
        v4u a[8];
        const int ao = l15 < 8 ? pos * 2048 + (w >> 1) * 1024 + ((w & 1) * 32 + g4) * 16 : 0x7ffffff0;
#pragma unroll
        for (int k = 0; k < 8; ++k) a[k] = __builtin_amdgcn_raw_buffer_load_b128(rH, ao, k * 64, AUX_SC1);
        const int tok = (t >> 2) & 7, ptok = 2047 - (int)(smw[8 + tok] & 2047u);
        const u32 st = __builtin_amdgcn_raw_buffer_load_b32(rS, (ptok >> 1) * 64 + (ptok & 1) * 4, 0, AUX_SC1);
        f4 acc[4] = {zero4, zero4, zero4, zero4};
        const u32 ones = l15 == 8 ? 0x3f803f80u : 0u;
#pragma unroll
        for (int k = 0; k < 8; ++k) { const v4u f = a[k] | ones;
#pragma unroll
            for (int ct = 0; ct < 4; ++ct) acc[ct] = mm16b(f, Bf[k][ct], acc[ct]); }
        STAMP(18);
        LAS float* pw = (LAS float*)(sm + L_R0) + w * 12 * 68;
        if (g4 < 3) {
#pragma unroll
            for (int ct = 0; ct < 4; ++ct)
#pragma unroll
                for (int i = 0; i < 4; ++i) pw[(4 * g4 + i) * 68 + ct * 16 + l15] = acc[ct][i];
        }
        bar();
        const int f0 = (t >> 5) * 8 + (t & 3) * 2;
        f2 sum = {0.f, 0.f}, cs = {0.f, 0.f};
#pragma unroll
        for (int ww = 0; ww < 4; ++ww) { LAS float* q = (LAS float*)(sm + L_R0) + ww * 12 * 68; sum += *(LAS f2*)(q + tok * 68 + f0); cs += *(LAS f2*)(q + 8 * 68 + f0); }
        const float m = blo(st), rs = 1.f / bhi(st);
        u32 o = pkb((sum[0] - m * cs[0]) * rs, (sum[1] - m * cs[1]) * rs);
        if (!(rank & 1)) { const float g0 = blo(o), g1 = bhi(o); o = pkb(g0 / (1.f + __expf(-g0)), g1 / (1.f + __expf(-g1))); }
        __builtin_amdgcn_raw_buffer_store_b32(o, rA, ((grp * 2 + (rank & 1)) * 7 + (rank >> 1)) * 1024 + t * 4, 0, AUX_SC1);
        drain();
        if (l < 4) { GAS u32* fa = (GAS u32*)(ws + WS_FLAG2 + (grp * 4 + l) * 256 + (rank * 4 + w) * 4); asm volatile("global_atomic_swap %0, %1, off sc1" :: "v"(fa), "v"(tag) : "memory"); }
        STAMP(19);
        store_out();
    } else if (roleD) {
        store_out();
        const int cgp = rank - 14, tok = t >> 5, c2 = (t & 31) * 2;
        const u32 ktok = smw[8 + tok]; const int ptok = 2047 - (int)(ktok & 2047u);
        const int dcol = cgp * 64 + c2, da = dcol >> 5, pa = (da & 0x13) | ((da & 4) << 1) | ((da & 8) >> 1);
        const u32 hv = __builtin_amdgcn_raw_buffer_load_b32(rH, ptok * 2048 + ((dcol >> 4) & 1) * 1024 + (((dcol >> 3) & 1) * 32 + pa) * 16 + (dcol & 7) * 2, 0, AUX_SC1);
        const u32 st = __builtin_amdgcn_raw_buffer_load_b32(rS, (ptok >> 1) * 64 + (ptok & 1) * 4, 0, AUX_SC1);
        const u32 kb = ktok & 0xffff0000u; const float rsel = u2f((kb & 0x80000000u) ? (kb & 0x7fff0000u) : (~kb & 0xffff0000u));
        const float sg = rbf(1.f / (1.f + __expf(-rsel)));
        const float m = blo(st), rs = 1.f / bhi(st);
        const u32 cc = pkb(blo(hv) - m, bhi(hv) - m), ln = pkb(blo(cc) * rs, bhi(cc) * rs);
        drain();
        bar();
        STAMP(18);
        v4u a[4];
#pragma unroll
        for (int k = 0; k < 4; ++k) { const int ks = w + 4 * k;
            a[k] = __builtin_amdgcn_raw_buffer_load_b128(rA, ks < 14 ? (grp * 2 + (l15 >> 3)) * 7168 + ks * 512 + g4 * 128 + (l & 7) * 16 : 0x7ffffff0, 0, AUX_SC1); }
        f4 acc[4] = {zero4, zero4, zero4, zero4};
#pragma unroll
        for (int k = 0; k < 4; ++k) { v4u f;
#pragma unroll
            for (int d = 0; d < 4; ++d) { const float lo = blo(a[k][d]), hi = bhi(a[k][d]); f[d] = pkb(lo * dppf<0x128>(lo), hi * dppf<0x128>(hi)); }
#pragma unroll
            for (int ct = 0; ct < 4; ++ct) acc[ct] = mm16b(f, Bf[k][ct], acc[ct]); }
        STAMP(19);
        LAS float* pw = (LAS float*)(sm + L_R0) + w * 8 * 68;
        if (g4 < 2) {
#pragma unroll
            for (int ct = 0; ct < 4; ++ct)
#pragma unroll
                for (int i = 0; i < 4; ++i) pw[(4 * g4 + i) * 68 + ct * 16 + l15] = acc[ct][i];
        }
        bar();
        f2 sum = {0.f, 0.f};
#pragma unroll
        for (int ww = 0; ww < 4; ++ww) sum += *(LAS f2*)((LAS float*)(sm + L_R0) + ww * 8 * 68 + tok * 68 + c2);
        GAS u64* ap = (GAS u64*)(ws + WS_ACC) + (cgp * 8 + tok) * 64 + (t & 31);
        const float lim = 1.0e12f;
        const u64 i0 = (u64)(long long)(fminf(fmaxf(sum[0], -lim), lim) * 65536.f) + (1ull << 60) + (1ull << 56);
        const u64 i1 = (u64)(long long)(fminf(fmaxf(sum[1], -lim), lim) * 65536.f) + (1ull << 60) + (1ull << 56);
        u64 o0, o1;
        asm volatile("global_atomic_add_x2 %0, %2, %3, off sc0 sc1\n\tglobal_atomic_add_x2 %1, %2, %4, off offset:256 sc0 sc1\n\ts_waitcnt vmcnt(0)" : "=&v"(o0), "=&v"(o1) : "v"(ap), "v"(i0), "v"(i1) : "memory");
        o0 += i0; o1 += i1;
        const int srow = (ptok >> 3) + 256 * (((ptok >> 1) & 3) + 4 * (ptok & 1));
        GAS u16* op = (GAS u16*)out + srow * D_ + dcol;
        if ((o0 >> 60) == 8) { const float y = rbf(rbf((float)(long long)(o0 - (8ull << 60) - (8ull << 56)) * (1.f / 65536.f)) * sg); op[0] = (u16)(pkb(blo(hv) + rbf(blo(ln) + y), 0.f) & 0xffffu); }
        if ((o1 >> 60) == 8) { const float y = rbf(rbf((float)(long long)(o1 - (8ull << 60) - (8ull << 56)) * (1.f / 65536.f)) * sg); op[1] = (u16)(pkb(bhi(hv) + rbf(bhi(ln) + y), 0.f) & 0xffffu); }
        STAMP(20);
    } else {
        store_out();
    }
    (void)part;
    STAMP(21);
}
}

extern "C" void kernel_launch(void* const* d_in, const int* in_sizes, int n_in, void* d_out, int out_size, void* d_ws, size_t ws_size, hipStream_t stream) {
    (void)in_sizes; (void)n_in; (void)out_size; (void)ws_size;
    (void)hipFuncSetAttribute((const void*)fused, hipFuncAttributeMaxDynamicSharedMemorySize, L_TOTAL);
    fused<<<256, 384, L_TOTAL, stream>>>((const u16*)d_in[0], (const u16*)d_in[1], (const u16*)d_in[2], (const u16*)d_in[4],
                                         (const u16*)d_in[5], (const u16*)d_in[6], (const u16*)d_in[7], (u16*)d_out, (char*)d_ws);
}
```

```cpp
#include <hip/hip_runtime.h>
#include <stdint.h>

typedef unsigned short u16;
typedef unsigned u32;
typedef unsigned long long u64;
typedef u32 v4u __attribute__((ext_vector_type(4)));
typedef u32 v2u __attribute__((ext_vector_type(2)));
typedef float f2 __attribute__((ext_vector_type(2)));
typedef float f4 __attribute__((ext_vector_type(4)));
typedef float f16v __attribute__((ext_vector_type(16)));
typedef _Float16 h2 __attribute__((ext_vector_type(2)));
typedef _Float16 h4 __attribute__((ext_vector_type(4)));
typedef _Float16 h8 __attribute__((ext_vector_type(8)));
typedef __bf16 b2 __attribute__((ext_vector_type(2)));
typedef __bf16 b8 __attribute__((ext_vector_type(8)));
typedef short s4 __attribute__((ext_vector_type(4)));
#define DI __device__ __forceinline__
#define GAS __attribute__((address_space(1)))
#define LAS __attribute__((address_space(3)))
#define BC(T, x) __builtin_bit_cast(T, x)

#define LDELAY 8
#define WA 4
#define GDELAY 7
#define FA 16
#define FB 32
#define FC 44
#define FD 60
#define FE 84

namespace {
constexpr int S_ = 2048, D_ = 1024, DF_ = 513, F_ = 3584;
constexpr size_t WS_EPOCH = 0;
constexpr size_t WS_FLAG = 4096;
constexpr size_t FLAG_SYNC = 32 * 16 * 256;
constexpr size_t WS_KEY = WS_FLAG + 2 * FLAG_SYNC;
constexpr size_t WS_FLAG2 = WS_KEY + 32 * 32 * 256;
constexpr size_t WS_RK = WS_FLAG2 + 8 * 4 * 256;
constexpr size_t WS_ST = WS_RK + 4096;
constexpr size_t WS_ACT = WS_ST + 1024 * 64;
constexpr size_t WS_ACC = WS_ACT + 8 * 2 * 7 * 1024;
constexpr size_t WS_E = 1 << 20;
constexpr size_t WS_H = WS_E + (4 << 20);
constexpr size_t WS_TS = WS_H + (4 << 20);
static_assert(WS_ACC + 65536 <= WS_E, "map");
constexpr int L_W = 0;
constexpr int L_R1 = 131072 - 2 * 17408;
constexpr int L_WF = 131072 - 17408;
constexpr int L_R0 = 131072;
constexpr int L_SM = L_R0 + 17408;
constexpr int L_TW = L_SM + 256;
constexpr int L_JUNK = L_TW + 8192;
constexpr int L_TOTAL = L_JUNK + 256;
constexpr u32 SPIN_CAP = 1u << 16;
constexpr int CODE_BYTES = 23 * 1024;

DI float u2f(u32 x) { return BC(float, x); }
DI u32 f2u(float x) { return BC(u32, x); }
DI u32 pkh(float a, float b) { f2 v = {a, b}; return BC(u32, __builtin_convertvector(v, h2)); }
DI u32 pkb(float a, float b) { f2 v = {a, b}; return BC(u32, __builtin_convertvector(v, b2)); }
DI u32 rotp(u32 m) { const h2 v = BC(h2, m), k = {(_Float16)-1.f, (_Float16)1.f}; const h2 r = v.yx * k; return BC(u32, r); }
DI u32 rotm(u32 m) { const h2 v = BC(h2, m), k = {(_Float16)1.f, (_Float16)-1.f}; const h2 r = v.yx * k; return BC(u32, r); }
DI float rbf(float a) { return u2f(pkb(a, a) << 16); }
DI float blo(u32 d) { return u2f(d << 16); }
DI float bhi(u32 d) { return u2f(d & 0xffff0000u); }
DI float dot2(u32 a, u32 b, float c) { return __builtin_amdgcn_fdot2_f32_bf16(BC(b2, a), BC(b2, b), c, false); }
DI f16v mm32h(v4u a, v4u b, f16v c) { return __builtin_amdgcn_mfma_f32_32x32x16_f16(BC(h8, a), BC(h8, b), c, 0, 0, 0); }
DI f16v mm32b(v4u a, v4u b, f16v c) { return __builtin_amdgcn_mfma_f32_32x32x16_bf16(BC(b8, a), BC(b8, b), c, 0, 0, 0); }
DI f4 mm16h(v4u a, v4u b, f4 c) { return __builtin_amdgcn_mfma_f32_16x16x32_f16(BC(h8, a), BC(h8, b), c, 0, 0, 0); }
DI f4 mm16b(v4u a, v4u b, f4 c) { return __builtin_amdgcn_mfma_f32_16x16x32_bf16(BC(b8, a), BC(b8, b), c, 0, 0, 0); }
DI f4 mm16k16(v2u a, v2u b, f4 c) { return __builtin_amdgcn_mfma_f32_16x16x16f16(BC(h4, a), BC(h4, b), c, 0, 0, 0); }
DI v2u trd(LAS char* p) { return BC(v2u, __builtin_amdgcn_ds_read_tr16_b64_v4i16((LAS s4*)p)); }
DI void bar() { asm volatile("s_waitcnt lgkmcnt(0)\n\ts_barrier" ::: "memory"); }
DI void drain() { asm volatile("s_waitcnt vmcnt(0)" ::: "memory"); }
DI float sinr(float r) { return __builtin_amdgcn_sinf(r); }
DI float cosr(float r) { return __builtin_amdgcn_cosf(r); }
DI float rcpf(float x) { return __builtin_amdgcn_rcpf(x); }
DI float sigm(float x) { return rcpf(1.f + __builtin_amdgcn_exp2f(-1.44269504f * x)); }
template <int CTRL> DI float dppf(float v) { return u2f((u32)__builtin_amdgcn_update_dpp(0, (int)f2u(v), CTRL, 0xf, 0xf, true)); }
template <int CTRL> DI u32 dppu(u32 v) { return (u32)__builtin_amdgcn_update_dpp(0, (int)v, CTRL, 0xf, 0xf, true); }
DI float rowsum(float v) { v += dppf<0x128>(v); v += dppf<0x124>(v); v += dppf<0x122>(v); v += dppf<0x121>(v); return v; }
DI float swapadd32(float a, float b) { auto r = __builtin_amdgcn_permlane32_swap(f2u(a), f2u(b), false, false); const u32 x = r[0], y = r[1]; return u2f(x) + u2f(y); }
DI float swapadd16(float a, float b) { auto r = __builtin_amdgcn_permlane16_swap(f2u(a), f2u(b), false, false); const u32 x = r[0], y = r[1]; return u2f(x) + u2f(y); }
DI float rdl(float v, int lane) { return u2f((u32)__builtin_amdgcn_readlane((int)f2u(v), lane)); }
DI u32 rdlu(u32 v, int lane) { return (u32)__builtin_amdgcn_readlane((int)v, lane); }
DI float red4(float a, float b, float c, float d) { return rowsum(swapadd16(swapadd32(a, b), swapadd32(c, d))); }
DI u32 med3(u32 a, u32 b, u32 c) { return max(min(a, b), min(max(a, b), c)); }
DI int swap23(int v) { return (v & 0x13) | ((v & 4) << 1) | ((v & 8) >> 1); }
#define RSRC(p, n) __builtin_amdgcn_make_buffer_rsrc((void*)(p), 0, (n), 0x00020000)
#define PIN(x) asm volatile("" : "+v"(x))
#define SB() __builtin_amdgcn_sched_barrier(0)
constexpr int AUX_NT = 2, AUX_SC1 = 16;

#define STAMP(i) do {} while (0)
#define STAMP_DECL do {} while (0)
#define STAMP_OUT do {} while (0)

DI void ln_scalars(float red, float& mean, float& sd) {
    const float sq = u2f((u32)__builtin_amdgcn_permlane16_swap(f2u(red), f2u(red), false, false)[1]);
    mean = red * (1.f / 1024.f);
    const float var = fmaxf(sq * (1.f / 1024.f) - mean * mean, 0.f);
    sd = rbf(__builtin_amdgcn_sqrtf(rbf(rbf(var) + 1e-5f)));
}

DI void grid_sync(char* base, int p, int rep, u32 tag, int l) {
    if (l < 32) { GAS u32* a = (GAS u32*)(base + l * 4096 + (p >> 4) * 256 + (p & 15) * 4); asm volatile("global_atomic_swap %0, %1, off sc1" :: "v"(a), "v"(tag) : "memory"); }
    GAS u64* p0 = (GAS u64*)(base + rep * 4096 + (l >> 3) * 256 + (l & 7) * 8);
    const u64 want = ((u64)tag << 32) | tag, zero = 0;
    u64 v0, v1, m0, m1; u32 cap = SPIN_CAP;
    asm volatile("s_mov_b64 %2, exec\n\ts_mov_b64 %3, exec\n"
                 ".Lp%=:\n\t"
                 "s_mov_b64 exec, %2\n\ts_cbranch_execz .La%=\n\t"
                 "global_atomic_add_x2 %0, %5, %6, off sc0 sc1\n"
                 ".La%=:\n\t"
                 "s_mov_b64 exec, %3\n\ts_cbranch_execz .Lb%=\n\t"
                 "global_atomic_add_x2 %1, %5, %6, off offset:2048 sc0 sc1\n"
                 ".Lb%=:\n\t"
                 "s_mov_b64 exec, -1\n\t"
                 "s_sub_u32 %4, %4, 1\n\t"
                 "s_waitcnt vmcnt(0)\n\t"
                 "s_cbranch_scc1 .Ld%=\n\t"
                 "v_cmp_ne_u64 %2, %0, %7\n\tv_cmp_ne_u64 %3, %1, %7\n\t"
                 "s_or_b64 vcc, %2, %3\n\t"
                 "s_cbranch_scc1 .Lp%=\n"
                 ".Ld%=:"
                 : "=&v"(v0), "=&v"(v1), "=&s"(m0), "=&s"(m1), "+s"(cap) : "v"(p0), "v"(zero), "v"(want) : "memory", "vcc", "scc");
}

__global__ void __launch_bounds__(384) fused(const u16* __restrict__ x, const u16* __restrict__ fwr, const u16* __restrict__ fwi, const u16* __restrict__ wrt,
                                             const u16* __restrict__ wg, const u16* __restrict__ wu, const u16* __restrict__ wd, u16* __restrict__ out, char* __restrict__ ws) {
    extern __shared__ __attribute__((aligned(16))) char smem[];
    const int p = (blockIdx.x & 7) * 32 + (blockIdx.x >> 3);
    const int t = threadIdx.x, w = __builtin_amdgcn_readfirstlane(t >> 6), l = t & 63;
    const int grp = p >> 5, rank = p & 31;
    const bool roleG = rank < 14, roleD = rank >= 14 && rank < 30;
    LAS char* sm = (LAS char*)smem;
    LAS u32* smw = (LAS u32*)(sm + L_SM);
    STAMP_DECL;
    STAMP(0);
    u64 pc0; asm volatile("s_getpc_b64 %0" : "=s"(pc0));

    if (__builtin_expect(w < 4, 1)) {
    const int hh = l >> 5, pl = swap23(l & 31);
    const int l15 = l & 15, g4 = l >> 4;
    auto rx = RSRC(x, S_ * D_ * 2);
    v4u xr[2][2];
    { const int vo = (p + 256 * w) * 2048 + 64 * pl + 16 * hh;
      xr[0][0] = __builtin_amdgcn_raw_buffer_load_b128(rx, vo, 0, AUX_NT);
      xr[1][0] = __builtin_amdgcn_raw_buffer_load_b128(rx, vo, 1024 * 2048, AUX_NT);
      xr[0][1] = __builtin_amdgcn_raw_buffer_load_b128(rx, vo + 32, 0, AUX_NT);
      xr[1][1] = __builtin_amdgcn_raw_buffer_load_b128(rx, vo + 32, 1024 * 2048, AUX_NT); }
    __builtin_amdgcn_sched_barrier(0);

    v4u M1[4], M2[4]; f16v twc, tws;
    { const float plf = (float)pl * (1.f / 1024.f), b0 = plf * (float)(8 * hh), pl32 = plf * 32.f, b32 = b0 * 32.f;
      const u32 sgn = (pl & 1) ? 0x80008000u : 0u;
#pragma unroll
      for (int r = 0; r < 8; ++r) {
        const float a32 = pl32 * (float)r + b32, c = cosr(a32), s = sinr(a32);
        const u32 m1 = pkh(c, s), m2 = rotp(m1);
        M1[r >> 2][r & 3] = m1; M2[r >> 2][r & 3] = m2; M1[2 + (r >> 2)][r & 3] = m1 ^ sgn; M2[2 + (r >> 2)][r & 3] = m2 ^ sgn;
      }
#pragma unroll
      for (int r = 0; r < 16; ++r) { const float a1k = plf * (float)(16 * (r >> 3) + (r & 7)) + b0; twc[r] = cosr(a1k); tws[r] = sinr(a1k); }
      twc *= (1.f / 32.f); tws *= (1.f / 32.f); }
    v4u PI[2];
    { const int k = pl - 8 * hh; const u32 one = (k & 1) ? 0x3f800000u : 0x3f80u;
#pragma unroll
      for (int d = 0; d < 8; ++d) PI[d >> 2][d & 3] = (k >> 1) == 8 * (d >> 2) + (d & 3) ? one : 0u; }
    v2u D8f;
    {
      const float k = 0.35355339f;
      { const int q = l15 >> 1; const float e8 = (q & 1) ? -0.125f : 0.125f, th = (float)(q * g4) * 0.125f + ((l15 & 1) ? 0.25f : 0.f), es = (q & 1) ? -k : k;
        D8f[0] = pkh(k * cosr(th + e8), es * cosr(th - e8)); D8f[1] = pkh(k * cosr(th - e8), es * cosr(th + e8)); }
    }
#pragma unroll
    for (int i = 0; i < 4; ++i) { PIN(M1[i]); PIN(M2[i]); }
    PIN(twc); PIN(tws); PIN(PI[0]); PIN(PI[1]); PIN(D8f);
    if (w == 0) {
#pragma unroll
        for (int i = 0; i < 4; ++i) { *(LAS f4*)(sm + L_TW + i * 2048 + l * 16) = f4{twc[4 * i], twc[4 * i + 1], twc[4 * i + 2], twc[4 * i + 3]};
                                      *(LAS f4*)(sm + L_TW + i * 2048 + 1024 + l * 16) = f4{tws[4 * i], tws[4 * i + 1], tws[4 * i + 2], tws[4 * i + 3]}; }
    }
    f16v zero16;
#pragma unroll
    for (int i = 0; i < 16; ++i) zero16[i] = 0.f;
    const f4 zero4 = {0.f, 0.f, 0.f, 0.f};
    auto rE = RSRC(ws + WS_E, 4 << 20);
    int eoff = p * 16384 + g4 * 4096 + (128 * w + 4 * l15) * 4;
    int zlo = L_R0 + w * 4112 + (pl + 256 * hh - 1) * 8, zhi = L_R0 + w * 4112 + (511 - pl - 256 * hh) * 8 + 4 - 1792, zh0 = L_R0 + w * 4112 + (((pl + 256 * hh - 1) * 8) & 4095);
    int zrd = L_R0 + g4 * 4112 + (128 * w + 4 * l15) * 8;
    PIN(eoff); PIN(zlo); PIN(zhi); PIN(zh0); PIN(zrd);
    __builtin_amdgcn_sched_barrier(0);
    STAMP(1);

    {
        float s1a = 0.f, s2a = 0.f, s1b = 0.f, s2b = 0.f;
#pragma unroll
        for (int i = 0; i < 8; ++i) { const u32 a = xr[0][i >> 2][i & 3], b = xr[1][i >> 2][i & 3];
            s1a = dot2(a, 0x3f803f80u, s1a); s2a = dot2(a, a, s2a); s1b = dot2(b, 0x3f803f80u, s1b); s2b = dot2(b, b, s2b); }
        STAMP(2);
        f16v xa = mm32b(xr[0][0], PI[0], zero16); f16v xb = mm32b(xr[1][0], PI[0], zero16);
        xa = mm32b(xr[0][1], PI[1], xa); xb = mm32b(xr[1][1], PI[1], xb);
        float mean, sd; ln_scalars(red4(s1a, s1b, s2a, s2b), mean, sd);
        const float rs = rcpf(sd), of = -mean * rs;
        const float ra = rdl(rs, 0), rb = rdl(rs, 32), oa = rdl(of, 0), ob = rdl(of, 32);
        xa = xa * ra + oa; xb = xb * rb + ob;
        v4u z[4];
#pragma unroll
        for (int r = 0; r < 16; ++r) z[r >> 2][r & 3] = pkh(xa[r], xb[r]);
        f16v tr = zero16, ti = zero16;
#pragma unroll
        for (int s = 0; s < 4; ++s) { tr = mm32h(z[s], M1[s], tr); ti = mm32h(z[s], M2[s], ti); }
        const f16v ur = tr * twc + ti * tws, ui = ti * twc - tr * tws;
#pragma unroll
        for (int r = 0; r < 16; ++r) z[r >> 2][r & 3] = pkh(ur[r], ui[r]);
        tr = zero16; ti = zero16;
#pragma unroll
        for (int s = 0; s < 4; ++s) { tr = mm32h(M1[s], z[s], tr); ti = mm32h(M2[s], z[s], ti); }
        u32 zz[16];
#pragma unroll
        for (int r = 0; r < 16; ++r) zz[r] = pkh(tr[r], ti[r]);
        if (l == 0) zz[0] = zz[8];
        *(LAS u32*)(sm + zh0) = zz[0];
#pragma unroll
        for (int r = 1; r < 8; ++r) *(LAS u32*)(sm + zlo + 256 * r) = zz[r];
#pragma unroll
        for (int r = 0; r < 8; ++r) *(LAS u32*)(sm + zhi + 1792 - 256 * r) = zz[8 + r];
        STAMP(23);
        bar();
        STAMP(3);
        v4u q[4];
#pragma unroll
        for (int i = 0; i < 4; ++i) q[i] = *(LAS v4u*)(sm + zrd + (i >> 1) * 512 + (i & 1) * 16);
        SB();
        f4 d[8];
#pragma unroll
        for (int i = 0; i < 8; ++i) d[i] = mm16k16(D8f, v2u{q[i >> 1][2 * (i & 1)], q[i >> 1][2 * (i & 1) + 1]}, zero4);
#pragma unroll
        for (int u = 0; u < 2; ++u) {
            const v4u o0 = {pkh(d[4 * u][0], d[4 * u][1]), pkh(d[4 * u + 1][0], d[4 * u + 1][1]), pkh(d[4 * u + 2][0], d[4 * u + 2][1]), pkh(d[4 * u + 3][0], d[4 * u + 3][1])};
            const v4u o1 = {pkh(d[4 * u][2], d[4 * u][3]), pkh(d[4 * u + 1][2], d[4 * u + 1][3]), pkh(d[4 * u + 2][2], d[4 * u + 2][3]), pkh(d[4 * u + 3][2], d[4 * u + 3][3])};
            __builtin_amdgcn_raw_buffer_store_b128(o0, rE, eoff + u * 256, 0, AUX_SC1);
            __builtin_amdgcn_raw_buffer_store_b128(o1, rE, eoff + u * 256 + 2048, 0, AUX_SC1);
        }
        __builtin_amdgcn_sched_barrier(0);
        STAMP(4);
    }
    v4u wq[2];
    {
        v4u A1[2], A4[2], A2[4][2], A3[4][2];
        { float jb = (float)(32 * w + grp + 128 * l15) * (1.f / 2048.f); PIN(jb);
#pragma unroll
          for (int i = 0; i < 4; ++i) { const float bf = (float)(4 * g4 + i), a0 = bf * jb;
#pragma unroll
            for (int tau = 0; tau < 4; ++tau) { const float a = bf * (float)(tau * (8.f / 2048.f)) + a0, c = cosr(a), s = sinr(a); const u32 m = pkh(c, s); A2[tau][0][i] = m; A2[tau][1][i] = rotp(m); } }
          const float c8 = (float)(8 * l15 + grp) * (1.f / 128.f);
#pragma unroll
          for (int i = 0; i < 4; ++i) { const float a = (float)(4 * g4 + i) * c8, c = cosr(a) * 0.125f, s = sinr(a) * 0.125f; const u32 m = pkh(c, s); A1[0][i] = m; A1[1][i] = rotp(m); } }
        PIN(A1[0]); PIN(A1[1]);
#pragma unroll
        for (int tau = 0; tau < 4; ++tau) { PIN(A2[tau][0]); PIN(A2[tau][1]); }
        __builtin_amdgcn_sched_barrier(0);
        drain();
        bar();
        STAMP(5);
        { float bf = (float)l15 * (1.f / 2048.f); PIN(bf); const float a0 = bf * (float)(32 * w + grp + 512 * g4);
#pragma unroll
          for (int i = 0; i < 4; ++i)
#pragma unroll
            for (int tau = 0; tau < 4; ++tau) { const float a = bf * (float)(8 * tau + 128 * i) + a0, c = cosr(a), s = sinr(a); const u32 m = pkh(s, c); A3[tau][0][i] = rotm(m); A3[tau][1][i] = m; }
          const float b8 = bf * 16.f, a8 = b8 * (float)(32 * g4 + grp);
#pragma unroll
          for (int i = 0; i < 4; ++i) { const float a = b8 * (float)(8 * i) + a8, c = cosr(a) * 0.125f, s = sinr(a) * 0.125f; const u32 m = pkh(s, c); A4[0][i] = rotm(m); A4[1][i] = m; } }
        PIN(A4[0]); PIN(A4[1]);
        int goff = (64 * g4 + 4 * w + ((l >> 2) & 3)) * 16384 + p * 64 + (l & 3) * 16;
        int a1w0 = L_R1 + l15 * 1040 + g4 * 4 + w * 16, a1w1 = L_R1 + l15 * 1040 + g4 * 4 + (w ^ 2) * 16;
        int a1r = L_R1 + 4 * w * 1040 + l15 * 64 + (g4 ^ ((l15 >> 3) << 1)) * 16;
        int a2w = L_R0 + 4 * g4 * 1088 + l15 * 64 + (w ^ ((l15 >> 1) & 3) ^ (l15 >> 3)) * 16;
        const int tq = l & 3, sl = g4 ^ ((2 * tq) & 3) ^ (tq >> 1);
        int a2r0 = L_R0 + (4 * w + ((l >> 2) & 3)) * 1088 + tq * 256 + sl * 16, a2r1 = L_R0 + (4 * w + ((l >> 2) & 3)) * 1088 + tq * 256 + (sl ^ 1) * 16;
#pragma unroll
        for (int tau = 0; tau < 4; ++tau) { PIN(A3[tau][0]); PIN(A3[tau][1]); }
        PIN(goff); PIN(a1w0); PIN(a1w1); PIN(a1r); PIN(a2w); PIN(a2r0); PIN(a2r1);
        __builtin_amdgcn_sched_barrier(0);
        bar();
        STAMP(6);
        v4u v[4];
#pragma unroll
        for (int i = 0; i < 4; ++i) v[i] = __builtin_amdgcn_raw_buffer_load_b128(rE, goff, i * 16 * 16384, AUX_SC1);
        { auto rw = RSRC(wrt, 2048); const int o = 64 * pl + 16 * hh; wq[0] = __builtin_amdgcn_raw_buffer_load_b128(rw, o, 0, 0); wq[1] = __builtin_amdgcn_raw_buffer_load_b128(rw, o + 32, 0, 0); }
        __builtin_amdgcn_sched_barrier(0);
        f4 fr[4], fi[4];
        { LAS char* b = sm + L_WF + g4 * 4096 + (4 * w + (l15 >> 2)) * 32 + (l15 & 3) * 8;
#pragma unroll
          for (int i = 0; i < 4; ++i) { const v2u a = trd(b + (i >> 1) * 2048 + (i & 1) * 512), c = trd(b + (i >> 1) * 2048 + (i & 1) * 512 + 1024);
            fr[0][i] = blo(a[0]); fr[1][i] = bhi(a[0]); fr[2][i] = blo(a[1]); fr[3][i] = bhi(a[1]);
            fi[0][i] = blo(c[0]); fi[1][i] = bhi(c[0]); fi[2][i] = blo(c[1]); fi[3][i] = bhi(c[1]); } }
#pragma unroll
        for (int tau = 0; tau < 4; ++tau) { PIN(fr[tau]); PIN(fi[tau]); }
        __builtin_amdgcn_sched_barrier(0);
        f4 ar[4], ai[4];
#pragma unroll
        for (int e = 0; e < 4; ++e) { const v4u f = {v[0][e], v[1][e], v[2][e], v[3][e]}; ar[e] = mm16h(f, A1[0], zero4); ai[e] = mm16h(f, A1[1], zero4); }
        SB();
#pragma unroll
        for (int e = 0; e < 4; e += 2)
#pragma unroll
            for (int i = 0; i < 4; ++i) { LAS u32* d = (LAS u32*)(sm + (i < 2 ? a1w0 : a1w1) + (4 * i + e) * 64); d[0] = pkh(ar[e][i], ai[e][i]); d[16] = pkh(ar[e + 1][i], ai[e + 1][i]); }
        STAMP(7);
        bar();
#pragma unroll
        for (int tau = 0; tau < 4; ++tau) v[tau] = *(LAS v4u*)(sm + a1r + tau * 1040);
        SB();
#pragma unroll
        for (int tau = 0; tau < 4; ++tau) { ar[tau] = mm16h(A2[tau][0], v[tau], zero4); ai[tau] = mm16h(A2[tau][1], v[tau], zero4); }
        SB();
        f4 yr[4], yi[4];
#pragma unroll
        for (int tau = 0; tau < 4; ++tau) { yr[tau] = ar[tau] * fr[tau] - ai[tau] * fi[tau]; yi[tau] = ar[tau] * fi[tau] + ai[tau] * fr[tau]; }
        SB();
#pragma unroll
        for (int tau = 0; tau < 4; ++tau) { const v4u y = {pkh(yr[tau][0], yi[tau][0]), pkh(yr[tau][1], yi[tau][1]), pkh(yr[tau][2], yi[tau][2]), pkh(yr[tau][3], yi[tau][3])};
            ar[tau] = mm16h(A3[tau][0], y, zero4); ai[tau] = mm16h(A3[tau][1], y, zero4); }
        SB();
#pragma unroll
        for (int i = 0; i < 4; ++i) *(LAS v4u*)(sm + a2w + i * 1088) = v4u{pkh(ar[0][i], ai[0][i]), pkh(ar[1][i], ai[1][i]), pkh(ar[2][i], ai[2][i]), pkh(ar[3][i], ai[3][i])};
        bar();
        STAMP(8);
#pragma unroll
        for (int e = 0; e < 4; ++e) v[e] = *(LAS v4u*)(sm + (e < 2 ? a2r0 : a2r1) + e * 64);
        SB();
#pragma unroll
        for (int e = 0; e < 4; ++e) { ar[e] = mm16h(A4[0], v[e], zero4); ai[e] = mm16h(A4[1], v[e], zero4); }
        SB();
#pragma unroll
        for (int i = 0; i < 4; ++i) v[i] = v4u{pkh(ar[0][i], ai[0][i]), pkh(ar[1][i], ai[1][i]), pkh(ar[2][i], ai[2][i]), pkh(ar[3][i], ai[3][i])};
#pragma unroll
        for (int i = 0; i < 4; ++i) __builtin_amdgcn_raw_buffer_store_b128(v[i], rE, goff, i * 16 * 16384, AUX_SC1);
        __builtin_amdgcn_sched_barrier(0);
        STAMP(9);
    }
    f16v ha, hb;
#pragma unroll
    for (int r = 0; r < 16; r += 2) { const u32 a = xr[0][r >> 3][(r >> 1) & 3], b = xr[1][r >> 3][(r >> 1) & 3]; ha[r] = blo(a); ha[r + 1] = bhi(a); hb[r] = blo(b); hb[r + 1] = bhi(b); }
    float wsum;
    { float s = 0.f;
#pragma unroll
      for (int i = 0; i < 8; ++i) s = dot2(wq[i >> 2][i & 3], 0x3f803f80u, s);
      wsum = red4(s, s, s, s); }
    v2u D8i;
#pragma unroll
    for (int x2 = 0; x2 < 2; ++x2) { const int q = 2 * g4 + x2, j = (l15 & 3) ^ 1; const float e8 = x2 ? -0.125f : 0.125f, kk = 0.35355339f * 0.25f;
        const float th = (float)(q * (l15 >> 2)) * 0.125f + ((j == 0 || j == 3) ? e8 : -e8), sc = (x2 && (j & 1)) ? -kk : kk;
        D8i[x2] = pkh(sc * cosr(th), sc * cosr(th + 0.25f)); }
#pragma unroll
    for (int i = 0; i < 4; ++i) { const f4 c = *(LAS f4*)(sm + L_TW + i * 2048 + l * 16), s = *(LAS f4*)(sm + L_TW + i * 2048 + 1024 + l * 16);
#pragma unroll
        for (int j = 0; j < 4; ++j) { twc[4 * i + j] = c[j]; tws[4 * i + j] = s[j]; } }
    PIN(D8i); PIN(twc); PIN(tws);
    float nyq = (w == 3 && l15 == 15) ? 0.5f : 0.f;
    PIN(nyq);
    int zw = L_R0 + g4 * 4096 + (128 * w + 4 * l15) * 4;
    int ylo = L_R0 + w * 4096 + (pl + 256 * hh - 1) * 4, yhi = L_R0 + w * 4096 + 2048 + (511 - pl - 256 * hh) * 4 - 896;
    int ho = (p * 8 + 2 * w) * 2048 + l * 16;
    PIN(ha); PIN(hb); PIN(wsum); PIN(zw); PIN(ylo); PIN(yhi); PIN(ho);
    __builtin_amdgcn_sched_barrier(0);
    drain();
    bar();
    STAMP(10);
    bar();
    STAMP(11);
    v4u hA[2], hB[2];
    {
        v4u v[2][2];
#pragma unroll
        for (int u = 0; u < 2; ++u) { v[u][0] = __builtin_amdgcn_raw_buffer_load_b128(rE, eoff + u * 256, 0, AUX_SC1); v[u][1] = __builtin_amdgcn_raw_buffer_load_b128(rE, eoff + u * 256 + 2048, 0, AUX_SC1); }
        f4 d[8];
#pragma unroll
        for (int i = 0; i < 8; ++i) d[i] = mm16k16(D8i, v2u{v[i >> 2][0][i & 3], v[i >> 2][1][i & 3]}, zero4);
        d[7][2] += nyq * (d[7][0] - d[7][2]); d[7][3] += nyq * (d[7][1] - d[7][3]);
#pragma unroll
        for (int u = 0; u < 2; ++u) {
            *(LAS v4u*)(sm + zw + u * 256) = v4u{pkh(d[4 * u][0], d[4 * u][1]), pkh(d[4 * u + 1][0], d[4 * u + 1][1]), pkh(d[4 * u + 2][0], d[4 * u + 2][1]), pkh(d[4 * u + 3][0], d[4 * u + 3][1])};
            *(LAS v4u*)(sm + zw + u * 256 + 2048) = v4u{pkh(d[4 * u][2], d[4 * u][3]), pkh(d[4 * u + 1][2], d[4 * u + 1][3]), pkh(d[4 * u + 2][2], d[4 * u + 2][3]), pkh(d[4 * u + 3][2], d[4 * u + 3][3])}; }
        STAMP(12);
        bar();
        v4u z[4];
        { v2u y0, y1, y2, y3, y4, y5, y6, y7;
          asm volatile("ds_read2_b32 %2, %8 offset0:128 offset1:160\n\tds_read2_b32 %3, %8 offset0:192 offset1:224\n\t"
                       "ds_read2_b32 %4, %9 offset0:224 offset1:192\n\tds_read2_b32 %5, %9 offset0:160 offset1:128\n\t"
                       "ds_read2_b32 %6, %9 offset0:96 offset1:64\n\tds_read2_b32 %7, %9 offset0:32 offset1:0\n\t"
                       "ds_read2_b32 %0, %8 offset0:0 offset1:32\n\tds_read2_b32 %1, %8 offset0:64 offset1:96\n\t"
                       "s_waitcnt lgkmcnt(0)"
                       : "=&v"(y0), "=&v"(y1), "=&v"(y2), "=&v"(y3), "=&v"(y4), "=&v"(y5), "=&v"(y6), "=&v"(y7) : "v"(ylo), "v"(yhi) : "memory");
          z[0] = v4u{l == 0 ? 0u : y0[0], y0[1], y1[0], y1[1]}; z[1] = v4u{y2[0], y2[1], y3[0], y3[1]}; z[2] = v4u{y4[0], y4[1], y5[0], y5[1]}; z[3] = v4u{y6[0], y6[1], y7[0], y7[1]}; }
        f16v tr = zero16, ti = zero16;
#pragma unroll
        for (int s = 0; s < 4; ++s) { const int s_ = (s + 1) & 3; tr = mm32h(z[s_], M1[s_], tr); ti = mm32h(z[s_], M2[s_], ti); }
        const f16v ur = tr * twc + ti * tws, ui = ti * twc - tr * tws;
#pragma unroll
        for (int r = 0; r < 16; ++r) z[r >> 2][r & 3] = pkh(ur[r], ui[r]);
#pragma unroll
        for (int s = 0; s < 4; ++s) { hb = mm32h(z[s], M1[s], hb); ha = mm32h(z[s], M2[s], ha); }
#pragma unroll
        for (int r = 0; r < 16; r += 2) { hA[r >> 3][(r >> 1) & 3] = pkb(ha[r], ha[r + 1]); hB[r >> 3][(r >> 1) & 3] = pkb(hb[r], hb[r + 1]); }
    }
    auto rH = RSRC(ws + WS_H, 4 << 20);
    __builtin_amdgcn_raw_buffer_store_b128(hA[0], rH, ho, 0, AUX_SC1); __builtin_amdgcn_raw_buffer_store_b128(hA[1], rH, ho + 1024, 0, AUX_SC1);
    __builtin_amdgcn_raw_buffer_store_b128(hB[0], rH, ho + 2048, 0, AUX_SC1); __builtin_amdgcn_raw_buffer_store_b128(hB[1], rH, ho + 3072, 0, AUX_SC1);
    __builtin_amdgcn_sched_barrier(0);
    STAMP(13);
    float mA, mB, rsA, rsB;
    auto rS = RSRC(ws + WS_ST, 65536);
    {
        float s1a = 0.f, s2a = 0.f, s3a = 0.f, s1b = 0.f, s2b = 0.f, s3b = 0.f;
#pragma unroll
        for (int i = 0; i < 8; ++i) { const u32 a = hA[i >> 2][i & 3], b = hB[i >> 2][i & 3], q = wq[i >> 2][i & 3];
            s1a = dot2(a, 0x3f803f80u, s1a); s2a = dot2(a, a, s2a); s3a = dot2(a, q, s3a); s1b = dot2(b, 0x3f803f80u, s1b); s2b = dot2(b, b, s2b); s3b = dot2(b, q, s3b); }
        const float s3h = swapadd32(s3a, s3b), red3 = rowsum(swapadd16(s3h, s3h));
        float mean, sd; ln_scalars(red4(s1a, s1b, s2a, s2b), mean, sd);
        const float m = rbf(mean), rs = rcpf(sd);
        const u32 rb = pkb((red3 - m * wsum) * rs, 0.f) & 0xffffu;
        const u32 key = (rb ^ ((rb & 0x8000u) ? 0xffffu : 0x8000u));
        if ((l & 31) == 0) {
            *(LAS u16*)(sm + L_SM + 16 + w * 4 + hh * 2) = (u16)key;
            __builtin_amdgcn_raw_buffer_store_b32((f2u(m) >> 16) | f2u(sd), rS, (p * 4 + w) * 64 + hh * 4, 0, AUX_SC1);
        }
        mA = rdl(m, 0); mB = rdl(m, 32); rsA = rdl(rs, 0); rsB = rdl(rs, 32);
    }
    STAMP(14);
    bar();
    v4u oA[2], oB[2];
#pragma unroll
    for (int s2 = 0; s2 < 2; ++s2)
#pragma unroll
        for (int d = 0; d < 4; ++d) {
            { const float h0 = blo(hA[s2][d]), h1 = bhi(hA[s2][d]); const u32 c = pkb(h0 - mA, h1 - mA), n = pkb(blo(c) * rsA, bhi(c) * rsA); oA[s2][d] = pkb(h0 + blo(n), h1 + bhi(n)); }
            if (s2 == 0) { const float h0 = blo(hB[s2][d]), h1 = bhi(hB[s2][d]); const u32 c = pkb(h0 - mB, h1 - mB), n = pkb(blo(c) * rsB, bhi(c) * rsB); oB[s2][d] = pkb(h0 + blo(n), h1 + bhi(n)); } }
    PIN(oA[0]); PIN(oA[1]); PIN(oB[0]);
    __builtin_amdgcn_sched_barrier(0);
    drain();
    bar();
    STAMP(15);
#pragma unroll
    for (int d = 0; d < 4; ++d) { const float h0 = blo(hB[1][d]), h1 = bhi(hB[1][d]); const u32 c = pkb(h0 - mB, h1 - mB), n = pkb(blo(c) * rsB, bhi(c) * rsB); oB[1][d] = pkb(h0 + blo(n), h1 + bhi(n)); }
    {
        LAS char* ob = sm + L_R0 + w * 4352;
#pragma unroll
        for (int s2 = 0; s2 < 2; ++s2) { const int o = 64 * pl + (((2 * s2 + hh) ^ (pl >> 1)) & 3) * 16; *(LAS v4u*)(ob + o) = oA[s2]; *(LAS v4u*)(ob + 2048 + o) = oB[s2]; }
        asm volatile("s_waitcnt lgkmcnt(0)" ::: "memory");
#pragma unroll
        for (int s2 = 0; s2 < 2; ++s2) { const int a_ = 16 * s2 + (l >> 2), o = 64 * a_ + (((l & 3) ^ (a_ >> 1)) & 3) * 16; oA[s2] = *(LAS v4u*)(ob + o); oB[s2] = *(LAS v4u*)(ob + 2048 + o); }
    }
    for (u32 sp = 0; sp < SPIN_CAP; ++sp) { if (*(volatile LAS u32*)smw) break; __builtin_amdgcn_s_sleep(1); }
    asm volatile("" ::: "memory");
    auto rA = RSRC(ws + WS_ACT, 8 * 2 * 7 * 1024);
    auto store_out = [&]() {
        const int pos = 2047 - (int)((smw[8 + (l & 7)] >> 5) & 2047u);
        const bool selA = __any(pos == p * 8 + 2 * w), selB = __any(pos == p * 8 + 2 * w + 1);
        auto ro = RSRC(out, S_ * D_ * 2); const int oo = (p + 256 * w) * 2048 + l * 16;
        if (!selA) { __builtin_amdgcn_raw_buffer_store_b128(oA[0], ro, oo, 0, AUX_SC1); __builtin_amdgcn_raw_buffer_store_b128(oA[1], ro, oo + 1024, 0, AUX_SC1); }
        if (!selB) { __builtin_amdgcn_raw_buffer_store_b128(oB[0], ro, oo, 1024 * 2048, AUX_SC1); __builtin_amdgcn_raw_buffer_store_b128(oB[1], ro, oo + 1024, 1024 * 2048, AUX_SC1); } };

    if (roleG) {
        v4u Bf[8][4];
        { const int sw = ((l15 >> 3) | (((w ^ g4) & 1) << 1)) * 32; LAS char* b = sm + L_W + (32 * g4 + 8 * w + (l15 >> 2)) * 128 + (l15 & 3) * 8;
#pragma unroll
          for (int ct = 0; ct < 4; ++ct) { LAS char* b2 = b + ((ct * 32) ^ sw);
#pragma unroll
            for (int k = 0; k < 8; ++k) { const v2u lo = trd(b2 + swap23(4 * k) * 4096), hi = trd(b2 + swap23(4 * k) * 4096 + 512); Bf[k][ct] = v4u{lo[0], lo[1], hi[0], hi[1]}; } } }
        __builtin_amdgcn_sched_barrier(0);
#pragma unroll
        for (int k = 0; k < 8; ++k) { PIN(Bf[k][0]); PIN(Bf[k][1]); PIN(Bf[k][2]); PIN(Bf[k][3]); }
        const u32 tag = smw[1];
        const int tok = (t >> 2) & 7;
        int xw = L_R0 + w * 4352 + (l >> 3) * 544 + (l & 7) * 16, xrd = (l15 < 8 ? L_R0 + w * 4352 + l15 * 544 : L_TW + w * 512) + g4 * 16;
        if (l < 32) *(LAS v4u*)(sm + L_TW + w * 512 + l * 16) = v4u{0x3f803f80u, 0x3f803f80u, 0x3f803f80u, 0x3f803f80u};
        int pwr = L_W + w * 2448 + l15 * 272 + g4 * 16, pcs = L_W + 8 * 272 + ((t >> 5) * 8 + (t & 3) * 2) * 4, prd = pcs + (tok - 8) * 272;
        PIN(xw); PIN(xrd); PIN(pwr); PIN(pcs); PIN(prd);
        STAMP(16);
        bar();
        STAMP(17);
        const int pos = 2047 - (int)((smw[8 + (l >> 3)] >> 5) & 2047u), ptok = 2047 - (int)((smw[8 + tok] >> 5) & 2047u);
        v4u a[8];
        const int ao = pos * 2048 + w * 512 + (l & 7) * 16;
#pragma unroll
        for (int k = 0; k < 4; ++k) a[k] = __builtin_amdgcn_raw_buffer_load_b128(rH, ao + k * 128, 0, AUX_SC1);
        const u32 st = __builtin_amdgcn_raw_buffer_load_b32(rS, (ptok >> 1) * 64 + (ptok & 1) * 4, 0, AUX_SC1);
#pragma unroll
        for (int k = 0; k < 4; ++k) *(LAS v4u*)(sm + xw + k * 128) = a[k];
        asm volatile("s_waitcnt lgkmcnt(0)" ::: "memory");
#pragma unroll
        for (int k = 0; k < 8; ++k) a[k] = *(LAS v4u*)(sm + xrd + k * 64);
        SB();
        f4 acc[4] = {zero4, zero4, zero4, zero4};
#pragma unroll
        for (int k = 0; k < 8; ++k)
#pragma unroll
            for (int ct = 0; ct < 4; ++ct) acc[ct] = mm16b(Bf[k][ct], a[k], acc[ct]);
        STAMP(18);
        if (l15 < 9) {
#pragma unroll
            for (int ct = 0; ct < 4; ++ct) *(LAS f4*)(sm + pwr + ct * 64) = acc[ct];
        }
        bar();
        f2 sum = {0.f, 0.f}, cs = {0.f, 0.f};
#pragma unroll
        for (int ww = 0; ww < 4; ++ww) { sum += *(LAS f2*)(sm + prd + ww * 2448); cs += *(LAS f2*)(sm + pcs + ww * 2448); }
        const float m = blo(st), rs = rcpf(bhi(st));
        u32 o = pkb((sum[0] - m * cs[0]) * rs, (sum[1] - m * cs[1]) * rs);
        if (!(rank & 1)) { const float g0 = blo(o), g1 = bhi(o); o = pkb(g0 * sigm(g0), g1 * sigm(g1)); }
        __builtin_amdgcn_raw_buffer_store_b32(o, rA, ((grp * 2 + (rank & 1)) * 7 + (rank >> 1)) * 1024 + t * 4, 0, AUX_SC1);
        STAMP(19);
        drain();
        if (l < 4) { GAS u32* fa = (GAS u32*)(ws + WS_FLAG2 + (grp * 4 + l) * 256 + (rank * 4 + w) * 4); asm volatile("global_atomic_swap %0, %1, off sc1" :: "v"(fa), "v"(tag) : "memory"); }
        STAMP(20);
        store_out();
    } else if (roleD) {
        v4u Bf[4][4];
        { const int sw = ((l15 >> 3) | (((w ^ g4) & 1) << 1)) * 32; LAS char* b = sm + L_W + (32 * w + 8 * g4 + (l15 >> 2)) * 128 + (l15 & 3) * 8;
#pragma unroll
          for (int ct = 0; ct < 4; ++ct) { LAS char* b2 = b + ((ct * 32) ^ sw);
#pragma unroll
            for (int k = 0; k < 4; ++k) { LAS char* b3 = b2 + ((k < 3 || w < 2) ? k * 16384 : 0); const v2u lo = trd(b3), hi = trd(b3 + 512); Bf[k][ct] = v4u{lo[0], lo[1], hi[0], hi[1]}; } } }
        __builtin_amdgcn_sched_barrier(0);
#pragma unroll
        for (int k = 0; k < 4; ++k) { PIN(Bf[k][0]); PIN(Bf[k][1]); PIN(Bf[k][2]); PIN(Bf[k][3]); }
        bar();
        store_out();
        const int cgp = rank - 14, tok = t >> 5, c2 = (t & 31) * 2;
        const u32 ktok = smw[8 + tok]; const int ptok = 2047 - (int)((ktok >> 5) & 2047u);
        const int dcol = cgp * 64 + c2;
        const u32 hv = __builtin_amdgcn_raw_buffer_load_b32(rH, ptok * 2048 + ((dcol >> 3) & 3) * 512 + swap23(dcol >> 5) * 16 + (dcol & 7) * 2, 0, AUX_SC1);
        const u32 st = __builtin_amdgcn_raw_buffer_load_b32(rS, (ptok >> 1) * 64 + (ptok & 1) * 4, 0, AUX_SC1);
        const u32 kb = ktok & 0xffff0000u; const float rsel = u2f((kb & 0x80000000u) ? (kb & 0x7fff0000u) : (~kb & 0xffff0000u));
        float sg = rbf(sigm(rsel));
        const float m = blo(st), rs = rcpf(bhi(st));
        const u32 cc = pkb(blo(hv) - m, bhi(hv) - m); u32 ln = pkb(blo(cc) * rs, bhi(cc) * rs);
        int aoff = (grp * 2 + (l15 >> 3)) * 7168 + w * 512 + g4 * 128 + (l & 7) * 16, aoff3 = w < 2 ? aoff + 3 * 2048 : 0x7ffffff0;
        GAS u64* ap = (GAS u64*)(ws + WS_ACC) + (cgp * 8 + tok) * 64 + (t & 31);
        GAS u16* op = (GAS u16*)out + ((ptok >> 3) + 256 * (((ptok >> 1) & 3) + 4 * (ptok & 1))) * D_ + dcol;
        int pwr = L_W + w * 2176 + l15 * 272 + g4 * 16, prd = L_W + tok * 272 + c2 * 4;
        PIN(sg); PIN(ln); PIN(aoff); PIN(aoff3); PIN(ap); PIN(op); PIN(pwr); PIN(prd);
        drain();
        STAMP(17);
        bar();
        STAMP(18);
        v4u a[4];
#pragma unroll
        for (int k = 0; k < 3; ++k) a[k] = __builtin_amdgcn_raw_buffer_load_b128(rA, aoff, k * 2048, AUX_SC1);
        a[3] = __builtin_amdgcn_raw_buffer_load_b128(rA, aoff3, 0, AUX_SC1);
        f4 acc[4] = {zero4, zero4, zero4, zero4};
#pragma unroll
        for (int k = 0; k < 4; ++k) { v4u f;
#pragma unroll
            for (int d = 0; d < 4; ++d) { const float lo = blo(a[k][d]), hi = bhi(a[k][d]); f[d] = pkb(lo * dppf<0x128>(lo), hi * dppf<0x128>(hi)); }
#pragma unroll
            for (int ct = 0; ct < 4; ++ct) acc[ct] = mm16b(Bf[k][ct], f, acc[ct]); }
        STAMP(19);
        if (l15 < 8) {
#pragma unroll
            for (int ct = 0; ct < 4; ++ct) *(LAS f4*)(sm + pwr + ct * 64) = acc[ct];
        }
        bar();
        f2 sum = {0.f, 0.f};
#pragma unroll
        for (int ww = 0; ww < 4; ++ww) sum += *(LAS f2*)(sm + prd + ww * 2176);
        const float lim = 4.0e9f; const double magic = 103079215104.0;
        const u64 bias = (1ull << 60) + (1ull << 48) - BC(u64, magic);
        const u64 i0 = BC(u64, (double)__builtin_amdgcn_fmed3f(sum[0], -lim, lim) + magic) + bias;
        const u64 i1 = BC(u64, (double)__builtin_amdgcn_fmed3f(sum[1], -lim, lim) + magic) + bias;
        const u64 back = BC(u64, magic) - (8ull << 60) - (8ull << 48);
        u64 j0 = i0 + back, j1 = i1 + back;
        u64 o0, o1;
        STAMP(20);
        asm volatile("global_atomic_add_x2 %0, %2, %3, off sc0 sc1\n\tglobal_atomic_add_x2 %1, %2, %4, off offset:256 sc0 sc1\n\ts_waitcnt vmcnt(0)" : "=&v"(o0), "=&v"(o1) : "v"(ap), "v"(i0), "v"(i1) : "memory");
        STAMP(21);
        const u32 yy = pkb((float)(BC(double, o0 + j0) - magic), (float)(BC(double, o1 + j1) - magic)), ys = pkb(blo(yy) * sg, bhi(yy) * sg);
        const u32 ob = pkb(blo(ln) + blo(ys), bhi(ln) + bhi(ys)), res = pkb(blo(hv) + blo(ob), bhi(hv) + bhi(ob));
        if ((u32)(o0 >> 60) == 7) op[0] = (u16)res;
        if ((u32)(o1 >> 60) == 7) op[1] = (u16)(res >> 16);
    } else {
        bar();
        store_out();
    }
    STAMP(22);
    STAMP_OUT;
    } else if (w == 4) {
        const u16* wsrc; int rstride;
        if (roleG) { wsrc = ((rank & 1) ? wu : wg) + grp * 448 + (rank >> 1) * 64; rstride = F_; }
        else { wsrc = wd + (size_t)grp * 448 * D_ + (roleD ? (rank - 14) * 64 : 0); rstride = D_; }
        wsrc += (l >> 3) * rstride + ((l & 7) ^ ((l >> 3) & 2)) * 8;
        const int wtog = (l & 4) ? -32 : 32;
        const int nfill = roleG ? 128 : (roleD ? 56 : 0);
        int f = 0;
        auto fill = [&](int upto) { if (upto > nfill) upto = nfill;
#pragma unroll 1
            for (; f < upto; ++f) { __builtin_amdgcn_global_load_lds((const GAS void*)(wsrc + (((f ^ (f >> 2)) & 1) ? wtog : 0)), (LAS void*)(sm + L_W + f * 1024), 16, 0, AUX_NT); wsrc += 8 * rstride; } };
        __builtin_amdgcn_s_sleep(LDELAY);
        auto touch = [&](const u16* q) { __builtin_amdgcn_global_load_lds((const GAS void*)q, (LAS void*)(sm + L_JUNK), 2, 0, AUX_NT); };
        { const int o = (grp + 8 * (8 * rank + (l >> 3))) * DF_ + (l & 7) * 64;
          touch(fwr + o); touch(fwi + o);
          if ((l & 7) == 0) { touch(fwr + o + 512); touch(fwi + o + 512); } }
        int fo = (grp + 8 * (l >> 1)) * DF_ + 16 * rank + 1 + (l & 1) * 8, fb = 0;
        auto filt = [&](int upto) {
#pragma unroll 1
          for (; fb < upto; ++fb) {
            __builtin_amdgcn_global_load_lds((const GAS void*)(fwr + fo), (LAS void*)(sm + L_WF + fb * 2048), 16, 0, AUX_NT);
            __builtin_amdgcn_global_load_lds((const GAS void*)(fwi + fo), (LAS void*)(sm + L_WF + fb * 2048 + 1024), 16, 0, AUX_NT);
            fo += 256 * DF_; } };
        fill(FA);
        STAMP(10);
        if (WA > 0) { drain(); STAMP(11); filt(WA); }
        STAMP(1);
        bar();
        __builtin_amdgcn_s_sleep(GDELAY);
        filt(8);
        STAMP(9);
        bar();
        drain();
        STAMP(2);
        bar();
        __builtin_amdgcn_s_sleep(2);
        fill(FB);
        STAMP(3);
        bar();
        fill(FC);
        STAMP(4);
        bar();
        fill(FD);
        STAMP(5);
        bar();
        bar();
        __builtin_amdgcn_s_sleep(2);
        fill(FE);
        STAMP(6);
        bar();
        fill(128);
        STAMP(7);
        bar();
        bar();
        drain();
        STAMP(8);
        if (l == 0) *(volatile LAS u32*)smw = 1u;
        asm volatile("s_waitcnt lgkmcnt(0)" ::: "memory");
        STAMP_OUT;
    } else {
        const u32 tag = (u32)__builtin_amdgcn_raw_buffer_load_b32(RSRC(ws, 64), 0, 0, 0) + 1u;
        {
            const char* pc = (const char*)(pc0 & ~(u64)255) + l * 128;
#pragma unroll
            for (int i = 0; i < (CODE_BYTES + 8191) / 8192; ++i)
                if (i * 8192 + l * 128 < CODE_BYTES) __builtin_amdgcn_global_load_lds((const GAS void*)(pc + i * 8192), (LAS void*)(sm + L_JUNK), 4, 0, 0);
        }
        const int rep = blockIdx.x & 31;
        GAS u64* kslot = (GAS u64*)(ws + WS_KEY + (l & 31) * 8192 + (p >> 3) * 256 + (p & 7) * 8);
        { auto ra = RSRC(ws + WS_ACC, 65536); __builtin_amdgcn_raw_buffer_store_b32(0u, ra, p * 256 + l * 4, 0, AUX_SC1); }
        if (l < 32) { const u64 z = 0; asm volatile("global_atomic_swap_x2 %0, %1, off sc1" :: "v"(kslot), "v"(z) : "memory"); }
        bar();
        drain();
        if (l == 0) { smw[0] = 0u; smw[1] = tag; }
        bar();
        STAMP(1);
        grid_sync(ws + WS_FLAG, p, rep, tag, l);
        STAMP(2);
        bar();
        if (blockIdx.x == 0 && l == 0) *(u32*)(ws + WS_EPOCH) = tag;
        bar(); bar(); bar();
        STAMP(3);
        grid_sync(ws + WS_FLAG + FLAG_SYNC, p, rep, tag, l);
        STAMP(4);
        bar();
        bar(); bar();
        u64 mine;
        { const v4u kv = *(LAS v4u*)(smw + 4);
          if (l < 4) { GAS u32* a = (GAS u32*)(ws + WS_RK + (p * 4 + l) * 4); const u32 d = smw[4 + l]; asm volatile("global_atomic_swap %0, %1, off sc1" :: "v"(a), "v"(d) : "memory"); }
          u32 a = 0, b = 0, c = 0;
#pragma unroll
          for (int j = 0; j < 8; ++j) { const u32 k = ((j & 1) ? (kv[j >> 1] & 0xffff0000u) : (kv[j >> 1] << 16)) | (u32)((2047 - p * 8 - j) << 5);
              c = med3(b, c, k); b = med3(a, b, k); a = max(a, k); }
          const u32 dl = min((b >> 16) - (c >> 16), 511u);
          mine = ((u64)(b | (dl & 31u)) << 32) | (a | 16u | (dl >> 5)); }
        PIN(mine);
        drain();
        bar();
        STAMP(5);
        if (l < 32) asm volatile("global_atomic_swap_x2 %0, %1, off sc1" :: "v"(kslot), "v"(mine) : "memory");
        u64 v0, v1, v2, v3;
        { GAS u64* p0 = (GAS u64*)(ws + WS_KEY + rep * 8192 + 4096 + (l >> 3) * 256 + (l & 7) * 8); const u64 zero = 0;
          u64 m0, m1, m2, m3; u32 cap = SPIN_CAP;
          asm volatile("s_mov_b64 %4, exec\n\ts_mov_b64 %5, exec\n\ts_mov_b64 %6, exec\n\ts_mov_b64 %7, exec\n"
                 ".Lp%=:\n\t"
                 "s_mov_b64 exec, %4\n\ts_cbranch_execz .La%=\n\t"
                 "global_atomic_add_x2 %0, %9, %10, off offset:-4096 sc0 sc1\n"
                 ".La%=:\n\t"
                 "s_mov_b64 exec, %5\n\ts_cbranch_execz .Lb%=\n\t"
                 "global_atomic_add_x2 %1, %9, %10, off offset:-2048 sc0 sc1\n"
                 ".Lb%=:\n\t"
                 "s_mov_b64 exec, %6\n\ts_cbranch_execz .Lc%=\n\t"
                 "global_atomic_add_x2 %2, %9, %10, off sc0 sc1\n"
                 ".Lc%=:\n\t"
                 "s_mov_b64 exec, %7\n\ts_cbranch_execz .Le%=\n\t"
                 "global_atomic_add_x2 %3, %9, %10, off offset:2048 sc0 sc1\n"
                 ".Le%=:\n\t"
                 "s_mov_b64 exec, -1\n\t"
                 "s_sub_u32 %8, %8, 1\n\t"
                 "s_waitcnt vmcnt(0)\n\t"
                 "s_cbranch_scc1 .Ld%=\n\t"
                 "v_cmp_eq_u64 %4, %0, %10\n\tv_cmp_eq_u64 %5, %1, %10\n\tv_cmp_eq_u64 %6, %2, %10\n\tv_cmp_eq_u64 %7, %3, %10\n\t"
                 "s_or_b64 vcc, %4, %5\n\ts_or_b64 vcc, vcc, %6\n\ts_or_b64 vcc, vcc, %7\n\t"
                 "s_cbranch_scc1 .Lp%=\n"
                 ".Ld%=:"
                 : "=&v"(v0), "=&v"(v1), "=&v"(v2), "=&v"(v3), "=&s"(m0), "=&s"(m1), "=&s"(m2), "=&s"(m3), "+s"(cap) : "v"(p0), "v"(zero) : "memory", "vcc", "scc"); }
        STAMP(6);
        {
          const u32 f0 = (u32)v0, f1 = (u32)v1, f2_ = (u32)v2, f3 = (u32)v3, s0 = (u32)(v0 >> 32), s1 = (u32)(v1 >> 32), s2 = (u32)(v2 >> 32), s3 = (u32)(v3 >> 32);
          u32 a = f0, b = s0, c = 0;
#define INS(k) do { c = med3(b, c, k); b = med3(a, b, k); a = max(a, k); } while (0)
          INS(f1); INS(f2_); INS(f3); INS(s1); INS(s2); INS(s3);
          const u32 a16 = a >> 16, b16 = b >> 16;
          u32 gm = max(a16, dppu<0xb1>(a16)); gm = max(gm, dppu<0x4e>(gm)); gm = max(gm, dppu<0x141>(gm));
          const u32 ot = dppu<0x128>(gm), mx = max(gm, ot), mn = min(gm, ot);
          u32 lo;
          { u32 hi, mid, c1, c2; u64 n1;
            asm volatile("s_nop 0\n\t"
                 "v_readlane_b32 %0, %8, 0\n\tv_readlane_b32 %2, %8, 16\n\tv_readlane_b32 %3, %8, 32\n\tv_readlane_b32 %4, %8, 48\n\t"
                 "s_min_u32 %0, %0, %2\n\ts_min_u32 %3, %3, %4\n\tv_readlane_b32 %1, %9, 0\n\tv_readlane_b32 %2, %9, 16\n\ts_min_u32 %0, %0, %3\n\t"
                 "v_readlane_b32 %3, %9, 32\n\tv_readlane_b32 %4, %9, 48\n\t"
                 "s_max_u32 %1, %1, %2\n\ts_max_u32 %3, %3, %4\n\ts_max_u32 %1, %1, %3\n\ts_add_u32 %1, %1, 1\n"
                 ".Lq%=:\n\t"
                 "s_add_u32 %2, %0, %1\n\ts_lshr_b32 %2, %2, 1\n\t"
                 "v_cmp_le_u32 vcc, %2, %6\n\tv_cmp_le_u32 %5, %2, %7\n\t"
                 "s_cmp_eq_u32 %2, %0\n\ts_cbranch_scc1 .Lz%=\n\t"
                 "s_bcnt1_i32_b64 %3, vcc\n\ts_bcnt1_i32_b64 %4, %5\n\ts_add_u32 %3, %3, %4\n\t"
                 "s_cmp_ge_u32 %3, 8\n\ts_cselect_b32 %0, %2, %0\n\ts_cselect_b32 %1, %1, %2\n\t"
                 "s_cmp_lg_u32 %3, 8\n\ts_cbranch_scc1 .Lq%=\n"
                 ".Lz%=:"
                 : "=&s"(lo), "=&s"(hi), "=&s"(mid), "=&s"(c1), "=&s"(c2), "=&s"(n1) : "v"(a16), "v"(b16), "v"(mn), "v"(mx) : "vcc", "scc"); }
          const u64 ga = __ballot(a16 > lo), gb = __ballot(b16 > lo), ea = __ballot(a16 == lo), eb = __ballot(b16 == lo);
          const u32 n0 = __popcll(ga), n1_ = n0 + __popcll(gb), n2 = n1_ + __popcll(ea);
#define MBC(m, base) __builtin_amdgcn_mbcnt_hi((u32)((m) >> 32), __builtin_amdgcn_mbcnt_lo((u32)(m), (base)))
          const u32 ia = a16 > lo ? MBC(ga, 0) : MBC(ea, n1_), ib = b16 > lo ? MBC(gb, n0) : MBC(eb, n2);
          bool amb = (c >> 16) > lo;
          if (__builtin_expect(gb != 0, 0)) {
#define THIRD(f, s) ((((s) >> 16) - ((((f) & 15u) << 5) | ((s) & 31u))) > lo)
              amb = amb || THIRD(f0, s0) || THIRD(f1, s1) || THIRD(f2_, s2) || THIRD(f3, s3); }
          if (__builtin_expect(!__any(amb), 1)) {
              if (a16 >= lo && ia < 8) smw[8 + ia] = a;
              if (b16 >= lo && ib < 8) smw[8 + ib] = b;
          } else {
              auto rk = RSRC(ws + WS_RK, 4096);
              v4u kv[4];
#pragma unroll
              for (int i = 0; i < 4; ++i) kv[i] = __builtin_amdgcn_raw_buffer_load_b128(rk, (i * 64 + l) * 16, 0, AUX_SC1);
              u32 prev = 0xffffffffu;
#pragma unroll 1
              for (int n = 0; n < 8; ++n) { u32 best = 0;
#pragma unroll
                  for (int j = 0; j < 32; ++j) { const u32 d = kv[j >> 3][(j >> 1) & 3], k = ((j & 1) ? (d & 0xffff0000u) : (d << 16)) | (u32)((2047 - ((j >> 3) * 64 + l) * 8 - (j & 7)) << 5);
                      best = max(best, k < prev ? k : 0u); }
                  best = max(best, dppu<0x128>(best)); best = max(best, dppu<0x124>(best)); best = max(best, dppu<0x122>(best)); best = max(best, dppu<0x121>(best));
                  prev = max(max(rdlu(best, 0), rdlu(best, 16)), max(rdlu(best, 32), rdlu(best, 48)));
                  if (l == 0) smw[8 + n] = prev; }
          } }
        STAMP(7);
        bar();
        if (roleD) {
            GAS u32* pp = (GAS u32*)(ws + WS_FLAG2 + (grp * 4 + (rank & 3)) * 256 + l * 4);
            u32 v, cap = SPIN_CAP;
            asm volatile("s_lshr_b64 exec, -1, 8\n"
                 ".Lp%=:\n\t"
                 "global_atomic_add %0, %2, %3, off sc0 sc1\n\t"
                 "s_sub_u32 %1, %1, 1\n\t"
                 "s_waitcnt vmcnt(0)\n\t"
                 "s_cbranch_scc1 .Ld%=\n\t"
                 "v_cmp_ne_u32 vcc, %0, %4\n\t"
                 "s_and_b64 exec, exec, vcc\n\t"
                 "s_cbranch_scc1 .Lp%=\n"
                 ".Ld%=:\n\t"
                 "s_mov_b64 exec, -1"
                 : "=&v"(v), "+s"(cap) : "v"(pp), "v"(0u), "v"(tag) : "memory", "vcc", "scc");
            STAMP(8);
            bar();
        }
        STAMP_OUT;
    }
}
}

extern "C" void kernel_launch(void* const* d_in, const int* in_sizes, int n_in, void* d_out, int out_size, void* d_ws, size_t ws_size, hipStream_t stream) {
    (void)in_sizes; (void)n_in; (void)out_size; (void)ws_size;
    (void)hipFuncSetAttribute((const void*)fused, hipFuncAttributeMaxDynamicSharedMemorySize, L_TOTAL);
    fused<<<256, 384, L_TOTAL, stream>>>((const u16*)d_in[0], (const u16*)d_in[1], (const u16*)d_in[2], (const u16*)d_in[4],
                                         (const u16*)d_in[5], (const u16*)d_in[6], (const u16*)d_in[7], (u16*)d_out, (char*)d_ws);
}
```

```cpp
#include <hip/hip_runtime.h>
#include <stdint.h>

typedef unsigned short u16;
typedef unsigned u32;
typedef unsigned long long u64;
typedef u32 v4u __attribute__((ext_vector_type(4)));
typedef u32 v2u __attribute__((ext_vector_type(2)));
typedef float f2 __attribute__((ext_vector_type(2)));
typedef float f4 __attribute__((ext_vector_type(4)));
typedef float f16v __attribute__((ext_vector_type(16)));
typedef _Float16 h2 __attribute__((ext_vector_type(2)));
typedef _Float16 h4 __attribute__((ext_vector_type(4)));
typedef _Float16 h8 __attribute__((ext_vector_type(8)));
typedef __bf16 b2 __attribute__((ext_vector_type(2)));
typedef __bf16 b8 __attribute__((ext_vector_type(8)));
typedef short s4 __attribute__((ext_vector_type(4)));
#define DI __device__ __forceinline__
#define GAS __attribute__((address_space(1)))
#define LAS __attribute__((address_space(3)))
#define BC(T, x) __builtin_bit_cast(T, x)

#define LDELAY 12
#define GDELAY 7
#define WB 8
#define NS1 32
#define TS2 16
#define NS2 128
#define DB8 0
#define AS0 7
#define NDT 0
#define DEPTH 28
#define DEPTHD 28
#define STR_(x) #x
#define STR(x) STR_(x)

namespace {
constexpr int S_ = 2048, D_ = 1024, DF_ = 513, F_ = 3584;
constexpr size_t WS_EPOCH = 0;
constexpr size_t WS_F1 = 4096;
constexpr size_t WS_F2 = WS_F1 + 32 * 16 * 256;
constexpr size_t WS_KEY = WS_F2 + 32 * 8 * 256;
constexpr size_t WS_FLAG2 = WS_KEY + 32 * 32 * 256;
constexpr size_t WS_RK = WS_FLAG2 + 8 * 4 * 256;
constexpr size_t WS_ST = WS_RK + 4096;
constexpr size_t WS_ACT = WS_ST + 1024 * 64;
constexpr size_t WS_ACC = WS_ACT + 8 * 2 * 7 * 1024;
constexpr int EC = 16384 + 128;
constexpr size_t WS_E = 10 << 20;
constexpr size_t WS_H = 5 << 20;
constexpr size_t WS_TS = WS_H + (4 << 20);
static_assert(WS_ACC + 65536 <= WS_H, "map");
constexpr int RU = 17408 + 128;
constexpr int L_W = 0;
constexpr int L_R1 = 0;
constexpr int L_WF = 2 * RU;
constexpr int L_R2 = L_WF + 32 * 1056;
constexpr int L_R0 = 131072;
constexpr int L_SM = L_R0 + 17408;
constexpr int L_TW = L_SM + 256;
constexpr int L_JUNK = L_TW + 8192;
constexpr int L_TOTAL = L_JUNK + 256;
static_assert(L_R2 + 2 * RU <= L_R0 && L_TOTAL <= 163840, "lds");
constexpr u32 SPIN_CAP = 1u << 16;
constexpr int CODE_BYTES = 39 * 1024;

DI float u2f(u32 x) { return BC(float, x); }
DI u32 f2u(float x) { return BC(u32, x); }
DI u32 pkh(float a, float b) { f2 v = {a, b}; return BC(u32, __builtin_convertvector(v, h2)); }
DI u32 pkb(float a, float b) { f2 v = {a, b}; return BC(u32, __builtin_convertvector(v, b2)); }
DI u32 rotp(u32 m) { const h2 v = BC(h2, m), k = {(_Float16)-1.f, (_Float16)1.f}; const h2 r = v.yx * k; return BC(u32, r); }
DI u32 rotm(u32 m) { const h2 v = BC(h2, m), k = {(_Float16)1.f, (_Float16)-1.f}; const h2 r = v.yx * k; return BC(u32, r); }
DI float rbf(float a) { return u2f(pkb(a, a) << 16); }
DI float blo(u32 d) { return u2f(d << 16); }
DI float bhi(u32 d) { return u2f(d & 0xffff0000u); }
DI float dot2(u32 a, u32 b, float c) { return __builtin_amdgcn_fdot2_f32_bf16(BC(b2, a), BC(b2, b), c, false); }
DI f16v mm32h(v4u a, v4u b, f16v c) { return __builtin_amdgcn_mfma_f32_32x32x16_f16(BC(h8, a), BC(h8, b), c, 0, 0, 0); }
DI f16v mm32b(v4u a, v4u b, f16v c) { return __builtin_amdgcn_mfma_f32_32x32x16_bf16(BC(b8, a), BC(b8, b), c, 0, 0, 0); }
DI f4 mm16h(v4u a, v4u b, f4 c) { return __builtin_amdgcn_mfma_f32_16x16x32_f16(BC(h8, a), BC(h8, b), c, 0, 0, 0); }
DI f4 mm16b(v4u a, v4u b, f4 c) { return __builtin_amdgcn_mfma_f32_16x16x32_bf16(BC(b8, a), BC(b8, b), c, 0, 0, 0); }
DI f4 mm16k16(v2u a, v2u b, f4 c) { return __builtin_amdgcn_mfma_f32_16x16x16f16(BC(h4, a), BC(h4, b), c, 0, 0, 0); }
DI v2u trd(LAS char* p) { return BC(v2u, __builtin_amdgcn_ds_read_tr16_b64_v4i16((LAS s4*)p)); }
DI void bar() { asm volatile("s_waitcnt lgkmcnt(0)\n\ts_barrier" ::: "memory"); }
DI void drain() { asm volatile("s_waitcnt vmcnt(0)" ::: "memory"); }
DI float sinr(float r) { return __builtin_amdgcn_sinf(r); }
DI float cosr(float r) { return __builtin_amdgcn_cosf(r); }
DI float rcpf(float x) { return __builtin_amdgcn_rcpf(x); }
DI float sigm(float x) { return rcpf(1.f + __builtin_amdgcn_exp2f(-1.44269504f * x)); }
template <int CTRL> DI float dppf(float v) { return u2f((u32)__builtin_amdgcn_update_dpp(0, (int)f2u(v), CTRL, 0xf, 0xf, true)); }
template <int CTRL> DI u32 dppu(u32 v) { return (u32)__builtin_amdgcn_update_dpp(0, (int)v, CTRL, 0xf, 0xf, true); }
DI float rowsum(float v) { v += dppf<0x128>(v); v += dppf<0x124>(v); v += dppf<0x122>(v); v += dppf<0x121>(v); return v; }
DI float swapadd32(float a, float b) { auto r = __builtin_amdgcn_permlane32_swap(f2u(a), f2u(b), false, false); const u32 x = r[0], y = r[1]; return u2f(x) + u2f(y); }
DI float swapadd16(float a, float b) { auto r = __builtin_amdgcn_permlane16_swap(f2u(a), f2u(b), false, false); const u32 x = r[0], y = r[1]; return u2f(x) + u2f(y); }
DI float rdl(float v, int lane) { return u2f((u32)__builtin_amdgcn_readlane((int)f2u(v), lane)); }
DI u32 rdlu(u32 v, int lane) { return (u32)__builtin_amdgcn_readlane((int)v, lane); }
DI float red4(float a, float b, float c, float d) { return rowsum(swapadd16(swapadd32(a, b), swapadd32(c, d))); }
DI u32 med3(u32 a, u32 b, u32 c) { return max(min(a, b), min(max(a, b), c)); }
DI int swap23(int v) { return (v & 0x13) | ((v & 4) << 1) | ((v & 8) >> 1); }
#define RSRC(p, n) __builtin_amdgcn_make_buffer_rsrc((void*)(p), 0, (n), 0x00020000)
#define PIN(x) asm volatile("" : "+v"(x))
#define SB() __builtin_amdgcn_sched_barrier(0)
constexpr int AUX_NT = 0, AUX_SC1 = 16;

#define STAMP(i) do {} while (0)
#define STAMP_DECL do {} while (0)
#define STAMP_OUT do {} while (0)

DI void ln_scalars(float red, float& mean, float& sd) {
    const float sq = u2f((u32)__builtin_amdgcn_permlane16_swap(f2u(red), f2u(red), false, false)[1]);
    mean = red * (1.f / 1024.f);
    const float var = fmaxf(sq * (1.f / 1024.f) - mean * mean, 0.f);
    sd = rbf(__builtin_amdgcn_sqrtf(rbf(rbf(var) + 1e-5f)));
}

DI v4u mkrsrc(const void* p, u32 n) { const u64 a = (u64)p; return v4u{(u32)a, (u32)(a >> 32) & 0xffffu, n, 0x00020000u}; }
DI void dma16(v4u r, u32 voff, u32 soff, u32 lds) { asm volatile("s_mov_b32 m0, %3\n\ts_nop 0\n\tbuffer_load_dwordx4 %0, %1, %2 offen lds" :: "v"(voff), "s"(r), "s"(soff), "s"(lds) : "memory"); }
DI void touch(v4u r, u32 voff, u32 soff, u32 lds) { asm volatile("s_mov_b32 m0, %3\n\ts_nop 0\n\tbuffer_load_ushort %0, %1, %2 offen lds" :: "v"(voff), "s"(r), "s"(soff), "s"(lds) : "memory"); }

struct SyncOps { GAS u32* a; GAS u64* p0; u64 want, zero; u32 tag; };
template <int NP> DI SyncOps sync_ops(char* base, int id, int rep, u32 tag, int l) {
    SyncOps o; o.a = (GAS u32*)(base + (l & 31) * (NP * 2048) + (id >> 4) * 256 + (id & 15) * 4); o.p0 = (GAS u64*)(base + rep * (NP * 2048) + (l >> 3) * 256 + (l & 7) * 8);
    o.want = ((u64)tag << 32) | tag; o.zero = 0; o.tag = tag;
    PIN(o.a); PIN(o.p0); PIN(o.want); PIN(o.zero); PIN(o.tag);
    return o;
}
DI void sync_arrive(const SyncOps& o) {
    asm volatile("s_mov_b32 exec_hi, 0\n\ts_waitcnt lgkmcnt(0)\n\ts_barrier\n\tglobal_atomic_swap %0, %1, off sc1\n\ts_mov_b64 exec, -1" :: "v"(o.a), "v"(o.tag) : "memory");
}
template <int NAP> DI void sync_wait1(const SyncOps& o) {
    u64 v0; u32 cap = SPIN_CAP;
    asm volatile(".Lp%=:\n\t"
                 "global_atomic_add_x2 %0, %2, %3, off sc0 sc1\n\t"
                 "s_sub_u32 %1, %1, 1\n\ts_waitcnt vmcnt(0)\n\ts_cbranch_scc1 .Ld%=\n\t"
                 "v_cmp_ne_u64 vcc, %0, %4\n\ts_and_b64 exec, exec, vcc\n\ts_cbranch_scc0 .Ld%=\n\ts_sleep %5\n\ts_branch .Lp%=\n"
                 ".Ld%=:\n\ts_mov_b64 exec, -1"
                 : "=&v"(v0), "+s"(cap) : "v"(o.p0), "v"(o.zero), "v"(o.want), "i"(NAP) : "memory", "vcc", "scc");
}
template <int NAP> DI void sync_wait2(const SyncOps& o) {
    u64 v0, v1, m0, m1; u32 cap = SPIN_CAP;
    asm volatile("s_mov_b64 %2, -1\n\ts_mov_b64 %3, -1\n"
                 ".Lp%=:\n\t"
                 "s_mov_b64 exec, %2\n\ts_cbranch_execz .La%=\n\t"
                 "global_atomic_add_x2 %0, %5, %6, off sc0 sc1\n"
                 ".La%=:\n\t"
                 "s_mov_b64 exec, %3\n\ts_cbranch_execz .Lb%=\n\t"
                 "global_atomic_add_x2 %1, %5, %6, off offset:2048 sc0 sc1\n"
                 ".Lb%=:\n\t"
                 "s_mov_b64 exec, -1\n\t"
                 "s_sub_u32 %4, %4, 1\n\t"
                 "s_waitcnt vmcnt(0)\n\t"
                 "s_cbranch_scc1 .Ld%=\n\t"
                 "v_cmp_ne_u64 %2, %0, %7\n\tv_cmp_ne_u64 %3, %1, %7\n\t"
                 "s_or_b64 vcc, %2, %3\n\t"
                 "s_cbranch_scc0 .Ld%=\n\ts_sleep %8\n\ts_branch .Lp%=\n"
                 ".Ld%=:"
                 : "=&v"(v0), "=&v"(v1), "=&s"(m0), "=&s"(m1), "+s"(cap) : "v"(o.p0), "v"(o.zero), "v"(o.want), "i"(NAP) : "memory", "vcc", "scc");
}
struct Fill { v4u r; u32 a, b, so, m0, step; };
DI Fill fill_init(const u16* base, u32 rowbytes, u32 rows, int l) {
    Fill f; f.r = mkrsrc(base, (rows - 1) * rowbytes + 128u); f.a = (l >> 3) * rowbytes + ((l & 7) ^ ((l >> 3) & 2)) * 16; f.b = f.a ^ 64u;
    f.step = 8u * rowbytes; f.so = 0u - f.step; f.m0 = L_W - 1024; return f;
}
template <int DP> DI void fill(Fill& f, int n) {
    if (n > 0)
        asm volatile("s_mov_b32 m0, %3\n"
                 ".Lf%=:\n\t"
                 "s_add_u32 m0, m0, 0x400\n\ts_add_u32 %2, %2, %6\n\tbuffer_load_dwordx4 %0, %5, %2 offen lds\n\t"
                 "s_add_u32 m0, m0, 0x400\n\ts_add_u32 %2, %2, %6\n\tbuffer_load_dwordx4 %1, %5, %2 offen lds\n\t"
                 "s_add_u32 m0, m0, 0x400\n\ts_add_u32 %2, %2, %6\n\tbuffer_load_dwordx4 %0, %5, %2 offen lds\n\t"
                 "s_add_u32 m0, m0, 0x400\n\ts_add_u32 %2, %2, %6\n\tbuffer_load_dwordx4 %1, %5, %2 offen lds\n\t"
                 "s_sub_u32 %4, %4, 1\n\tv_swap_b32 %0, %1\n\ts_waitcnt vmcnt(%7)\n\ts_cmp_lg_u32 %4, 0\n\ts_cbranch_scc1 .Lf%=\n\t"
                 "s_mov_b32 %3, m0"
                 : "+v"(f.a), "+v"(f.b), "+s"(f.so), "+s"(f.m0), "+s"(n) : "s"(f.r), "s"(f.step), "i"(DP) : "memory", "scc");
}

template <int NAP> DI void select8(char* ws, int rep, int l, LAS u32* smw) {
        u64 v0, v1, v2, v3;
        { GAS u64* p0 = (GAS u64*)(ws + WS_KEY + rep * 8192 + 4096 + (l >> 3) * 256 + (l & 7) * 8); const u64 zero = 0;
          u64 m0, m1, m2, m3; u32 cap = SPIN_CAP;
          asm volatile("s_mov_b64 %4, -1\n\ts_mov_b64 %5, -1\n\ts_mov_b64 %6, -1\n\ts_mov_b64 %7, -1\n"
                 ".Lp%=:\n\t"
                 "s_mov_b64 exec, %4\n\ts_cbranch_execz .La%=\n\t"
                 "global_atomic_add_x2 %0, %9, %10, off offset:-4096 sc0 sc1\n"
                 ".La%=:\n\t"
                 "s_mov_b64 exec, %5\n\ts_cbranch_execz .Lb%=\n\t"
                 "global_atomic_add_x2 %1, %9, %10, off offset:-2048 sc0 sc1\n"
                 ".Lb%=:\n\t"
                 "s_mov_b64 exec, %6\n\ts_cbranch_execz .Lc%=\n\t"
                 "global_atomic_add_x2 %2, %9, %10, off sc0 sc1\n"
                 ".Lc%=:\n\t"
                 "s_mov_b64 exec, %7\n\ts_cbranch_execz .Le%=\n\t"
                 "global_atomic_add_x2 %3, %9, %10, off offset:2048 sc0 sc1\n"
                 ".Le%=:\n\t"
                 "s_mov_b64 exec, -1\n\t"
                 "s_sub_u32 %8, %8, 1\n\t"
                 "s_waitcnt vmcnt(0)\n\t"
                 "s_cbranch_scc1 .Ld%=\n\t"
                 "v_cmp_eq_u64 %4, %0, %10\n\tv_cmp_eq_u64 %5, %1, %10\n\tv_cmp_eq_u64 %6, %2, %10\n\tv_cmp_eq_u64 %7, %3, %10\n\t"
                 "s_or_b64 vcc, %4, %5\n\ts_or_b64 vcc, vcc, %6\n\ts_or_b64 vcc, vcc, %7\n\t"
                 "s_cbranch_scc0 .Ld%=\n\ts_sleep %11\n\ts_branch .Lp%=\n"
                 ".Ld%=:"
                 : "=&v"(v0), "=&v"(v1), "=&v"(v2), "=&v"(v3), "=&s"(m0), "=&s"(m1), "=&s"(m2), "=&s"(m3), "+s"(cap) : "v"(p0), "v"(zero), "i"(NAP) : "memory", "vcc", "scc"); }
        {
          const u32 f0 = (u32)v0, f1 = (u32)v1, f2_ = (u32)v2, f3 = (u32)v3, s0 = (u32)(v0 >> 32), s1 = (u32)(v1 >> 32), s2 = (u32)(v2 >> 32), s3 = (u32)(v3 >> 32);
          u32 a = f0, b = s0, c = 0;
#define INS(k) do { c = med3(b, c, k); b = med3(a, b, k); a = max(a, k); } while (0)
          INS(f1); INS(f2_); INS(f3); INS(s1); INS(s2); INS(s3);
          const u32 a16 = a >> 16, b16 = b >> 16;
          u32 gm = max(a16, dppu<0xb1>(a16)); gm = max(gm, dppu<0x4e>(gm)); gm = max(gm, dppu<0x141>(gm));
          const u32 ot = dppu<0x128>(gm), mx = max(gm, ot), mn = min(gm, ot);
          u32 lo;
          { u32 hi, mid, c1, c2; u64 n1;
            asm volatile("s_nop 0\n\t"
                 "v_readlane_b32 %0, %8, 0\n\tv_readlane_b32 %2, %8, 16\n\tv_readlane_b32 %3, %8, 32\n\tv_readlane_b32 %4, %8, 48\n\t"
                 "s_min_u32 %0, %0, %2\n\ts_min_u32 %3, %3, %4\n\tv_readlane_b32 %1, %9, 0\n\tv_readlane_b32 %2, %9, 16\n\ts_min_u32 %0, %0, %3\n\t"
                 "v_readlane_b32 %3, %9, 32\n\tv_readlane_b32 %4, %9, 48\n\t"
                 "s_max_u32 %1, %1, %2\n\ts_max_u32 %3, %3, %4\n\ts_max_u32 %1, %1, %3\n\ts_add_u32 %1, %1, 1\n"
                 ".Lq%=:\n\t"
                 "s_add_u32 %2, %0, %1\n\ts_lshr_b32 %2, %2, 1\n\t"
                 "v_cmp_le_u32 vcc, %2, %6\n\tv_cmp_le_u32 %5, %2, %7\n\t"
                 "s_cmp_eq_u32 %2, %0\n\ts_cbranch_scc1 .Lz%=\n\t"
                 "s_bcnt1_i32_b64 %3, vcc\n\ts_bcnt1_i32_b64 %4, %5\n\ts_add_u32 %3, %3, %4\n\t"
                 "s_cmp_ge_u32 %3, 8\n\ts_cselect_b32 %0, %2, %0\n\ts_cselect_b32 %1, %1, %2\n\t"
                 "s_cmp_lg_u32 %3, 8\n\ts_cbranch_scc1 .Lq%=\n"
                 ".Lz%=:"
                 : "=&s"(lo), "=&s"(hi), "=&s"(mid), "=&s"(c1), "=&s"(c2), "=&s"(n1) : "v"(a16), "v"(b16), "v"(mn), "v"(mx) : "vcc", "scc"); }
          const u64 ga = __ballot(a16 > lo), gb = __ballot(b16 > lo), ea = __ballot(a16 == lo), eb = __ballot(b16 == lo);
          const u32 n0 = __popcll(ga), n1_ = n0 + __popcll(gb), n2 = n1_ + __popcll(ea);
#define MBC(m, base) __builtin_amdgcn_mbcnt_hi((u32)((m) >> 32), __builtin_amdgcn_mbcnt_lo((u32)(m), (base)))
          const u32 ia = a16 > lo ? MBC(ga, 0) : MBC(ea, n1_), ib = b16 > lo ? MBC(gb, n0) : MBC(eb, n2);
          bool amb = (c >> 16) > lo;
          if (__builtin_expect(gb != 0, 0)) {
#define THIRD(f, s) ((((s) >> 16) - ((((f) & 15u) << 5) | ((s) & 31u))) > lo)
              amb = amb || THIRD(f0, s0) || THIRD(f1, s1) || THIRD(f2_, s2) || THIRD(f3, s3); }
          if (__builtin_expect(!__any(amb), 1)) {
              if (a16 >= lo && ia < 8) smw[8 + ia] = a;
              if (b16 >= lo && ib < 8) smw[8 + ib] = b;
          } else {
              auto rk = RSRC(ws + WS_RK, 4096);
              v4u kv[4];
#pragma unroll
              for (int i = 0; i < 4; ++i) kv[i] = __builtin_amdgcn_raw_buffer_load_b128(rk, (i * 64 + l) * 16, 0, AUX_SC1);
              u32 prev = 0xffffffffu;
#pragma unroll 1
              for (int n = 0; n < 8; ++n) { u32 best = 0;
#pragma unroll
                  for (int j = 0; j < 32; ++j) { const u32 d = kv[j >> 3][(j >> 1) & 3], k = ((j & 1) ? (d & 0xffff0000u) : (d << 16)) | (u32)((2047 - ((j >> 3) * 64 + l) * 8 - (j & 7)) << 5);
                      best = max(best, k < prev ? k : 0u); }
                  best = max(best, dppu<0x128>(best)); best = max(best, dppu<0x124>(best)); best = max(best, dppu<0x122>(best)); best = max(best, dppu<0x121>(best));
                  prev = max(max(rdlu(best, 0), rdlu(best, 16)), max(rdlu(best, 32), rdlu(best, 48)));
                  if (l == 0) smw[8 + n] = prev; }
          } }
}

template <typename F0, typename F> DI void phase2(char* ws, LAS char* sm, int w8, int l, int grp, int P0, F0 mid0, F mid
    ) {
        const int l15 = l & 15, g4 = l >> 4; const f4 zero4 = {0.f, 0.f, 0.f, 0.f};
        auto rE = RSRC(ws + WS_E, 256 * EC);
        v4u A1[2], A4[2], A2[2][2][2], A3[2][2];
        { float jb = (float)(16 * w8 + grp + 128 * l15) * (1.f / 2048.f); PIN(jb);
#pragma unroll
          for (int i = 0; i < 4; ++i) { const float bf = (float)(4 * g4 + i), a0 = bf * jb;
#pragma unroll
            for (int tau = 0; tau < 2; ++tau) { const float a = bf * (float)(tau * (8.f / 2048.f)) + a0, c = cosr(a), s = sinr(a); const u32 m = pkh(c, s), n = pkh(-s, c);
                A2[tau][0][0][i] = m; A2[tau][0][1][i] = n; A2[tau][1][0][i ^ 1] = m; A2[tau][1][1][i ^ 1] = n; } }
          const float c8 = (float)(8 * l15 + grp) * (1.f / 128.f);
#pragma unroll
          for (int i = 0; i < 4; ++i) { const float a = (float)(4 * g4 + i) * c8, c = cosr(a) * 0.125f, s = sinr(a) * 0.125f; A1[0][i] = pkh(c, s); A1[1][i] = pkh(-s, c); }
          PIN(A1[0]); PIN(A1[1]);
#pragma unroll
          for (int tau = 0; tau < 2; ++tau) { PIN(A2[tau][0][0]); PIN(A2[tau][0][1]); PIN(A2[tau][1][0]); PIN(A2[tau][1][1]); }
          SB();
          mid0();
          float bf = (float)l15 * (1.f / 2048.f); PIN(bf); const float a0 = bf * (float)(16 * w8 + grp + 512 * g4);
#pragma unroll
          for (int i = 0; i < 4; ++i)
#pragma unroll
            for (int tau = 0; tau < 2; ++tau) { const float a = bf * (float)(8 * tau + 128 * i) + a0, c = cosr(a), s = sinr(a); A3[tau][0][i] = pkh(c, -s); A3[tau][1][i] = pkh(s, c); }
          const float b8 = bf * 16.f, a8 = b8 * (float)(32 * g4 + grp);
#pragma unroll
          for (int i = 0; i < 4; ++i) { const float a = b8 * (float)(8 * i) + a8, c = cosr(a) * 0.125f, s = sinr(a) * 0.125f; A4[0][i] = pkh(c, -s); A4[1][i] = pkh(s, c); } }
        int goff = (64 * g4 + 2 * w8 + ((l >> 3) & 1)) * EC + P0 * 64 + (l & 7) * 16;
        int a1w0 = L_R1 + (g4 & 1) * RU + l15 * 1040 + ((2 * (w8 & 1) + (g4 >> 1)) ^ (g4 & 1)) * 4 + (w8 >> 1) * 16, a1w1 = a1w0 + (((w8 >> 1) ^ 2) - (w8 >> 1)) * 16;
        int a1r = L_R1 + 2 * w8 * 1040 + l15 * 64 + (g4 ^ ((l15 >> 3) << 1)) * 16;
        int a2w = L_R2 + 4 * g4 * 1088 + l15 * 64 + ((w8 >> 1) ^ ((l15 >> 1) & 3) ^ (l15 >> 3)) * 16 + (w8 & 1) * 8;
        const int tq = l & 3, sl = g4 ^ ((2 * tq) & 3) ^ (tq >> 1);
        int a2r0 = L_R2 + ((l >> 2) & 1) * RU + (2 * w8 + ((l >> 3) & 1)) * 1088 + tq * 256 + sl * 16, a2r1 = a2r0 ^ 16;
        int fb = L_WF + (4 * g4 + (l15 >> 2)) * 1056 + 2 * w8 * 64 + (l15 & 3) * 8;
        PIN(A1[0]); PIN(A1[1]); PIN(A4[0]); PIN(A4[1]);
#pragma unroll
        for (int tau = 0; tau < 2; ++tau) { PIN(A2[tau][0][0]); PIN(A2[tau][0][1]); PIN(A2[tau][1][0]); PIN(A2[tau][1][1]); PIN(A3[tau][0]); PIN(A3[tau][1]); }
        PIN(goff); PIN(a1w0); PIN(a1w1); PIN(a1r); PIN(a2w); PIN(a2r0); PIN(a2r1); PIN(fb);
        SB();
        mid();
        STAMP(6);
        v4u v[4];
#pragma unroll
        for (int i = 0; i < 4; ++i) v[i] = __builtin_amdgcn_raw_buffer_load_b128(rE, goff, i * 16 * EC, AUX_SC1);
        SB();
        f4 fr[4], fi[4];
#pragma unroll
        for (int k = 0; k < 4; ++k) { const v2u a = trd(sm + fb + k * 32), c = trd(sm + fb + k * 32 + 16 * 1056);
            fr[k] = f4{blo(a[0]), bhi(a[0]), blo(a[1]), bhi(a[1])}; fi[k] = f4{blo(c[0]), bhi(c[0]), blo(c[1]), bhi(c[1])}; }
#pragma unroll
        for (int k = 0; k < 4; ++k) { PIN(fr[k]); PIN(fi[k]); }
        SB();
        f4 ar[4], ai[4];
#pragma unroll
        for (int e = 0; e < 4; ++e) { const v4u f = {v[0][e], v[1][e], v[2][e], v[3][e]}; ar[e] = mm16h(f, A1[0], zero4); ai[e] = mm16h(f, A1[1], zero4); }
        SB();
#pragma unroll
        for (int e = 0; e < 4; e += 2)
#pragma unroll
            for (int i = 0; i < 4; ++i) { LAS u32* d = (LAS u32*)(sm + (i < 2 ? a1w0 : a1w1) + (4 * i + e) * 64); d[0] = pkh(ar[e][i], ai[e][i]); d[16] = pkh(ar[e + 1][i], ai[e + 1][i]); }
        STAMP(7);
        bar();
#pragma unroll
        for (int k = 0; k < 4; ++k) v[k] = *(LAS v4u*)(sm + a1r + (k >> 1) * 1040 + (k & 1) * RU);
        SB();
#pragma unroll
        for (int k = 0; k < 4; ++k) { ar[k] = mm16h(A2[k >> 1][k & 1][0], v[k], zero4); ai[k] = mm16h(A2[k >> 1][k & 1][1], v[k], zero4); }
        SB();
        f4 yr[4], yi[4];
#pragma unroll
        for (int k = 0; k < 4; ++k) { yr[k] = ar[k] * fr[k] - ai[k] * fi[k]; yi[k] = ar[k] * fi[k] + ai[k] * fr[k]; }
        SB();
#pragma unroll
        for (int k = 0; k < 4; ++k) { const v4u y = {pkh(yr[k][0], yi[k][0]), pkh(yr[k][1], yi[k][1]), pkh(yr[k][2], yi[k][2]), pkh(yr[k][3], yi[k][3])};
            ar[k] = mm16h(A3[k >> 1][0], y, zero4); ai[k] = mm16h(A3[k >> 1][1], y, zero4); }
        SB();
#pragma unroll
        for (int u = 0; u < 2; ++u)
#pragma unroll
            for (int i = 0; i < 4; ++i) *(LAS v2u*)(sm + a2w + u * RU + i * 1088) = v2u{pkh(ar[u][i], ai[u][i]), pkh(ar[2 + u][i], ai[2 + u][i])};
        bar();
        STAMP(8);
#pragma unroll
        for (int e = 0; e < 4; ++e) v[e] = *(LAS v4u*)(sm + (e < 2 ? a2r0 : a2r1) + e * 64);
        SB();
#pragma unroll
        for (int e = 0; e < 4; ++e) { ar[e] = mm16h(A4[0], v[e], zero4); ai[e] = mm16h(A4[1], v[e], zero4); }
        SB();
#pragma unroll
        for (int i = 0; i < 4; ++i) v[i] = v4u{pkh(ar[0][i], ai[0][i]), pkh(ar[1][i], ai[1][i]), pkh(ar[2][i], ai[2][i]), pkh(ar[3][i], ai[3][i])};
#pragma unroll
        for (int i = 0; i < 4; ++i) __builtin_amdgcn_raw_buffer_store_b128(v[i], rE, goff, i * 16 * EC, AUX_SC1);
        SB();
        STAMP(9);
}
#define STARG

template <typename F> DI void down_phase(char* ws, u16* out, LAS char* sm, int w8, int l, int grp, int rk, F mid
    ) {
        const int l15 = l & 15, g4 = l >> 4, t = w8 * 64 + l; const f4 zero4 = {0.f, 0.f, 0.f, 0.f};
        LAS u32* smw = (LAS u32*)(sm + L_SM); const u32 tag = smw[1];
        auto rH = RSRC(ws + WS_H, 4 << 20); auto rS = RSRC(ws + WS_ST, 65536); auto rA = RSRC(ws + WS_ACT, 8 * 2 * 7 * 1024);
        const u32 ktok = smw[8 + w8]; const int ptok = 2047 - (int)((ktok >> 5) & 2047u);
        const int dcol = rk * 64 + l;
        const u32 hv = __builtin_amdgcn_raw_buffer_load_b16(rH, ptok * 2048 + ((dcol >> 3) & 3) * 512 + swap23(dcol >> 5) * 16 + (dcol & 7) * 2, 0, AUX_SC1);
        const u32 st = __builtin_amdgcn_raw_buffer_load_b32(rS, (ptok >> 1) * 64 + (ptok & 1) * 4, 0, AUX_SC1);
        SB();
        mid();
        SB();
        v4u Bf[2][4];
        { const int sw = ((l15 >> 3) | (((w8 ^ g4) & 1) << 1)) * 32; LAS char* b = sm + L_W + (32 * w8 + 8 * g4 + (l15 >> 2)) * 128 + (l15 & 3) * 8;
#pragma unroll
          for (int ct = 0; ct < 4; ++ct) { LAS char* b2 = b + ((ct * 32) ^ sw);
#pragma unroll
            for (int k = 0; k < 2; ++k) { LAS char* b3 = b2 + ((k == 1 && w8 < 6) ? 32768 : 0); const v2u lo = trd(b3), hi = trd(b3 + 512); Bf[k][ct] = v4u{lo[0], lo[1], hi[0], hi[1]}; } } }
        const u32 kb = ktok & 0xffff0000u; const float rsel = u2f((kb & 0x80000000u) ? (kb & 0x7fff0000u) : (~kb & 0xffff0000u));
        float sg = rbf(sigm(rsel));
        float hf = blo(hv), ln = rbf(rbf(hf - blo(st)) * rcpf(bhi(st)));
        int aoff = (grp * 2 + (l15 >> 3)) * 7168 + w8 * 512 + g4 * 128 + (l & 7) * 16, aoff1 = w8 < 6 ? aoff + 4096 : 0x7ffffff0;
        GAS u64* ap = (GAS u64*)(ws + WS_ACC) + rk * 512 + t;
        GAS u16* op = (GAS u16*)out + ((ptok >> 3) + 256 * (((ptok >> 1) & 3) + 4 * (ptok & 1))) * D_ + dcol;
        int pwr = L_R2 + w8 * 2176 + l15 * 272 + g4 * 16, prd = L_R2 + w8 * 272 + l * 4;
#pragma unroll
        for (int k = 0; k < 2; ++k) { PIN(Bf[k][0]); PIN(Bf[k][1]); PIN(Bf[k][2]); PIN(Bf[k][3]); }
        PIN(sg); PIN(ln); PIN(hf); PIN(aoff); PIN(aoff1); PIN(ap); PIN(op); PIN(pwr); PIN(prd);
        STAMP(17);
        if (w8 == 4) {
            GAS u32* pp = (GAS u32*)(ws + WS_FLAG2 + (grp * 4 + (rk & 3)) * 256 + l * 4);
            u32 v, cap = SPIN_CAP;
            asm volatile("s_lshr_b64 exec, -1, 8\n"
                 ".Lp%=:\n\t"
                 "global_atomic_add %0, %2, %3, off sc0 sc1\n\t"
                 "s_sub_u32 %1, %1, 1\n\t"
                 "s_waitcnt vmcnt(0)\n\t"
                 "s_cbranch_scc1 .Ld%=\n\t"
                 "v_cmp_ne_u32 vcc, %0, %4\n\t"
                 "s_and_b64 exec, exec, vcc\n\t"
                 "s_cbranch_scc1 .Lp%=\n"
                 ".Ld%=:\n\t"
                 "s_mov_b64 exec, -1"
                 : "=&v"(v), "+s"(cap) : "v"(pp), "v"(0u), "v"(tag) : "memory", "vcc", "scc");
        }
        bar();
        STAMP(18);
        v4u a[2];
        a[0] = __builtin_amdgcn_raw_buffer_load_b128(rA, aoff, 0, AUX_SC1);
        a[1] = __builtin_amdgcn_raw_buffer_load_b128(rA, aoff1, 0, AUX_SC1);
        f4 acc[4] = {zero4, zero4, zero4, zero4};
#pragma unroll
        for (int k = 0; k < 2; ++k) { v4u f;
#pragma unroll
            for (int d = 0; d < 4; ++d) { const float lo = blo(a[k][d]), hi = bhi(a[k][d]); f[d] = pkb(lo * dppf<0x128>(lo), hi * dppf<0x128>(hi)); }
#pragma unroll
            for (int ct = 0; ct < 4; ++ct) acc[ct] = mm16b(Bf[k][ct], f, acc[ct]); }
        STAMP(19);
        if (l15 < 8) {
#pragma unroll
            for (int ct = 0; ct < 4; ++ct) *(LAS f4*)(sm + pwr + ct * 64) = acc[ct];
        }
        bar();
        float ps[8];
#pragma unroll
        for (int ww = 0; ww < 8; ++ww) ps[ww] = *(LAS float*)(sm + prd + ww * 2176);
        const float sum = ((ps[0] + ps[1]) + (ps[2] + ps[3])) + ((ps[4] + ps[5]) + (ps[6] + ps[7]));
        const float lim = 4.0e9f; const double magic = 103079215104.0;
        const u64 i0 = BC(u64, (double)__builtin_amdgcn_fmed3f(sum, -lim, lim) + magic) + ((1ull << 60) + (1ull << 48) - BC(u64, magic));
        const u64 j0 = i0 + (BC(u64, magic) - (8ull << 60) - (8ull << 48));
        u64 o0;
        STAMP(20);
        asm volatile("global_atomic_add_x2 %0, %1, %2, off sc0 sc1\n\ts_waitcnt vmcnt(0)" : "=&v"(o0) : "v"(ap), "v"(i0) : "memory");
        const float y = rbf(rbf((float)(BC(double, o0 + j0) - magic)) * sg);
        if ((u32)(o0 >> 60) == 7) op[0] = (u16)pkb(hf + rbf(ln + y), 0.f);
}

__global__ void __launch_bounds__(512) fused(const u16* __restrict__ x, const u16* __restrict__ fwr, const u16* __restrict__ fwi, const u16* __restrict__ wrt,
                                             const u16* __restrict__ wg, const u16* __restrict__ wu, const u16* __restrict__ wd, u16* __restrict__ out, char* __restrict__ ws) {
    extern __shared__ __attribute__((aligned(16))) char smem[];
    const int t = threadIdx.x, w8 = __builtin_amdgcn_readfirstlane(t >> 6), l = t & 63;
    const int grp = blockIdx.x & 7, rk = blockIdx.x >> 4;
    const bool colwg = (blockIdx.x & 8) == 0;
    const int p = grp * 32 + (blockIdx.x >> 3), P0 = grp * 32 + 2 * rk;
    LAS char* sm = (LAS char*)smem;
    LAS u32* smw = (LAS u32*)(sm + L_SM);
    STAMP_DECL;
    STAMP(0);
    u32 cpf;
    { const int o = min((int)((blockIdx.x >> 3) + (t & 15) * 32) * 128, CODE_BYTES - 128);
      asm volatile("s_getpc_b64 vcc\n\ts_mov_b64 exec, 0x3ff\n\tglobal_load_dword %0, %1, vcc\n\ts_mov_b64 exec, -1" : "=&v"(cpf) : "v"(o) : "vcc"); }
    asm volatile("" :: "s"(x));

    if (__builtin_expect(w8 < 4, 1)) {
    const int w = w8, l15 = l & 15, g4 = l >> 4;
    const f4 zero4 = {0.f, 0.f, 0.f, 0.f};
    auto rE = RSRC(ws + WS_E, 256 * EC);
    __builtin_amdgcn_s_setprio(2);
    const int hh = l >> 5, pl = swap23(l & 31);
    auto rx = RSRC(x, S_ * D_ * 2);
    v4u xr[2][2];
    { const int vo = (p + 256 * w) * 2048 + 64 * pl + 16 * hh;
      xr[0][0] = __builtin_amdgcn_raw_buffer_load_b128(rx, vo, 0, AUX_NT);
      xr[1][0] = __builtin_amdgcn_raw_buffer_load_b128(rx, vo, 1024 * 2048, AUX_NT);
      xr[0][1] = __builtin_amdgcn_raw_buffer_load_b128(rx, vo + 32, 0, AUX_NT);
      xr[1][1] = __builtin_amdgcn_raw_buffer_load_b128(rx, vo + 32, 1024 * 2048, AUX_NT); }
    SB();
    v4u wq[2];
    { auto rw = RSRC(wrt, 2048); const int o = 64 * pl + 16 * hh; wq[0] = __builtin_amdgcn_raw_buffer_load_b128(rw, o, 0, 0); wq[1] = __builtin_amdgcn_raw_buffer_load_b128(rw, o + 32, 0, 0); }
    __builtin_amdgcn_sched_barrier(0);
    STAMP(21);

    v4u M1[4], M2[4]; f16v twc, tws;
    { const float plf = (float)pl * (1.f / 1024.f), b0 = plf * (float)(8 * hh), pl32 = plf * 32.f, b32 = b0 * 32.f;
      const u32 sgn = (pl & 1) ? 0x80008000u : 0u;
#pragma unroll
      for (int r = 0; r < 8; ++r) {
        const float a32 = pl32 * (float)r + b32, c = cosr(a32), s = sinr(a32);
        const u32 m1 = pkh(c, s), m2 = pkh(-s, c);
        M1[r >> 2][r & 3] = m1; M2[r >> 2][r & 3] = m2; M1[2 + (r >> 2)][r & 3] = m1 ^ sgn; M2[2 + (r >> 2)][r & 3] = m2 ^ sgn;
      }
#pragma unroll
      for (int r = 0; r < 16; ++r) { const float a1k = plf * (float)(16 * (r >> 3) + (r & 7)) + b0; twc[r] = cosr(a1k); tws[r] = sinr(a1k); }
      twc *= (1.f / 32.f); tws *= (1.f / 32.f); }
    v4u PI[2];
    { const int k = pl - 8 * hh; const u32 one = (k & 1) ? 0x3f800000u : 0x3f80u;
#pragma unroll
      for (int d = 0; d < 8; ++d) PI[d >> 2][d & 3] = (k >> 1) == 8 * (d >> 2) + (d & 3) ? one : 0u; }
    v2u D8f;
    {
      const float k = 0.35355339f;
      { const int q = l15 >> 1; const float e8 = (q & 1) ? -0.125f : 0.125f, th = (float)(q * g4) * 0.125f + ((l15 & 1) ? 0.25f : 0.f), es = (q & 1) ? -k : k;
        D8f[0] = pkh(k * cosr(th + e8), es * cosr(th - e8)); D8f[1] = pkh(k * cosr(th - e8), es * cosr(th + e8)); }
    }
#pragma unroll
    for (int i = 0; i < 4; ++i) { PIN(M1[i]); PIN(M2[i]); }
    PIN(twc); PIN(tws); PIN(PI[0]); PIN(PI[1]); PIN(D8f);
    if (w == 0) {
#pragma unroll
        for (int i = 0; i < 4; ++i) { *(LAS f4*)(sm + L_TW + i * 2048 + l * 16) = f4{twc[4 * i], twc[4 * i + 1], twc[4 * i + 2], twc[4 * i + 3]};
                                      *(LAS f4*)(sm + L_TW + i * 2048 + 1024 + l * 16) = f4{tws[4 * i], tws[4 * i + 1], tws[4 * i + 2], tws[4 * i + 3]}; }
    }
    f16v zero16;
#pragma unroll
    for (int i = 0; i < 16; ++i) zero16[i] = 0.f;
    int eoff = p * EC + g4 * 4096 + (128 * w + 4 * l15) * 4;
    int zlo = L_R0 + w * 4112 + (pl + 256 * hh - 1) * 8, zhi = L_R0 + w * 4112 + (511 - pl - 256 * hh) * 8 + 4 - 1792, zh0 = L_R0 + w * 4112 + (((pl + 256 * hh - 1) * 8) & 4095);
    int zrd = L_R0 + g4 * 4112 + (128 * w + 4 * l15) * 8;
    PIN(eoff); PIN(zlo); PIN(zhi); PIN(zh0); PIN(zrd);
    __builtin_amdgcn_sched_barrier(0);
    STAMP(1);

    {
        float s1a = 0.f, s2a = 0.f, s1b = 0.f, s2b = 0.f;
#pragma unroll
        for (int i = 0; i < 8; ++i) { const u32 a = xr[0][i >> 2][i & 3], b = xr[1][i >> 2][i & 3];
            s1a = dot2(a, 0x3f803f80u, s1a); s2a = dot2(a, a, s2a); s1b = dot2(b, 0x3f803f80u, s1b); s2b = dot2(b, b, s2b); }
        asm volatile("" :: "v"(cpf), "v"(s1a), "v"(s2b));
        STAMP(2);
        f16v xa = mm32b(xr[0][0], PI[0], zero16); f16v xb = mm32b(xr[1][0], PI[0], zero16);
        xa = mm32b(xr[0][1], PI[1], xa); xb = mm32b(xr[1][1], PI[1], xb);
        float mean, sd; ln_scalars(red4(s1a, s1b, s2a, s2b), mean, sd);
        const float rs = rcpf(sd), of = -mean * rs;
        const float ra = rdl(rs, 0), rb = rdl(rs, 32), oa = rdl(of, 0), ob = rdl(of, 32);
        xa = xa * ra + oa; xb = xb * rb + ob;
        v4u z[4];
#pragma unroll
        for (int r = 0; r < 16; ++r) z[r >> 2][r & 3] = pkh(xa[r], xb[r]);
        f16v tr = zero16, ti = zero16;
#pragma unroll
        for (int s = 0; s < 4; ++s) { tr = mm32h(z[s], M1[s], tr); ti = mm32h(z[s], M2[s], ti); }
        const f16v ur = tr * twc + ti * tws, ui = ti * twc - tr * tws;
#pragma unroll
        for (int r = 0; r < 16; ++r) z[r >> 2][r & 3] = pkh(ur[r], ui[r]);
        tr = zero16; ti = zero16;
#pragma unroll
        for (int s = 0; s < 4; ++s) { tr = mm32h(M1[s], z[s], tr); ti = mm32h(M2[s], z[s], ti); }
        u32 zz[16];
#pragma unroll
        for (int r = 0; r < 16; ++r) zz[r] = pkh(tr[r], ti[r]);
        if (l == 0) zz[0] = zz[8];
        *(LAS u32*)(sm + zh0) = zz[0];
#pragma unroll
        for (int r = 1; r < 8; ++r) *(LAS u32*)(sm + zlo + 256 * r) = zz[r];
#pragma unroll
        for (int r = 0; r < 8; ++r) *(LAS u32*)(sm + zhi + 1792 - 256 * r) = zz[8 + r];
        STAMP(23);
        bar();
        STAMP(3);
        v4u q[4];
#pragma unroll
        for (int i = 0; i < 4; ++i) q[i] = *(LAS v4u*)(sm + zrd + (i >> 1) * 512 + (i & 1) * 16);
        SB();
        f4 d[8];
#pragma unroll
        for (int i = 0; i < 8; ++i) d[i] = mm16k16(D8f, v2u{q[i >> 1][2 * (i & 1)], q[i >> 1][2 * (i & 1) + 1]}, zero4);
#pragma unroll
        for (int u = 0; u < 2; ++u) {
            const v4u o0 = {pkh(d[4 * u][0], d[4 * u][1]), pkh(d[4 * u + 1][0], d[4 * u + 1][1]), pkh(d[4 * u + 2][0], d[4 * u + 2][1]), pkh(d[4 * u + 3][0], d[4 * u + 3][1])};
            const v4u o1 = {pkh(d[4 * u][2], d[4 * u][3]), pkh(d[4 * u + 1][2], d[4 * u + 1][3]), pkh(d[4 * u + 2][2], d[4 * u + 2][3]), pkh(d[4 * u + 3][2], d[4 * u + 3][3])};
            __builtin_amdgcn_raw_buffer_store_b128(o0, rE, eoff + u * 256, 0, AUX_SC1);
            __builtin_amdgcn_raw_buffer_store_b128(o1, rE, eoff + u * 256 + 2048, 0, AUX_SC1);
        }
        __builtin_amdgcn_sched_barrier(0);
        STAMP(4);
    }
    __builtin_amdgcn_s_setprio(0);
    if (colwg) phase2(ws, sm, w8, l, grp, P0, [&]() { drain(); STAMP(5); bar(); }, [&]() { bar(); } STARG);
    else { drain(); STAMP(5); bar(); }
    f16v ha, hb;
#pragma unroll
    for (int r = 0; r < 16; r += 2) { const u32 a = xr[0][r >> 3][(r >> 1) & 3], b = xr[1][r >> 3][(r >> 1) & 3]; ha[r] = blo(a); ha[r + 1] = bhi(a); hb[r] = blo(b); hb[r + 1] = bhi(b); }
    float wsum;
    { float s = 0.f;
#pragma unroll
      for (int i = 0; i < 8; ++i) s = dot2(wq[i >> 2][i & 3], 0x3f803f80u, s);
      wsum = red4(s, s, s, s); }
    v2u D8i;
#pragma unroll
    for (int x2 = 0; x2 < 2; ++x2) { const int q = 2 * g4 + x2, j = (l15 & 3) ^ 1; const float e8 = x2 ? -0.125f : 0.125f, kk = 0.35355339f * 0.25f;
        const float th = (float)(q * (l15 >> 2)) * 0.125f + ((j == 0 || j == 3) ? e8 : -e8), sc = (x2 && (j & 1)) ? -kk : kk;
        D8i[x2] = pkh(sc * cosr(th), sc * cosr(th + 0.25f)); }
#pragma unroll
    for (int i = 0; i < 4; ++i) { const f4 c = *(LAS f4*)(sm + L_TW + i * 2048 + l * 16), s = *(LAS f4*)(sm + L_TW + i * 2048 + 1024 + l * 16);
#pragma unroll
        for (int j = 0; j < 4; ++j) { twc[4 * i + j] = c[j]; tws[4 * i + j] = s[j]; } }
    PIN(D8i); PIN(twc); PIN(tws);
    float nyq = (w == 3 && l15 == 15) ? 0.5f : 0.f;
    PIN(nyq);
    int zw = L_R0 + g4 * 4096 + (128 * w + 4 * l15) * 4;
    int ylo = L_R0 + w * 4096 + (pl + 256 * hh - 1) * 4, yhi = L_R0 + w * 4096 + 2048 + (511 - pl - 256 * hh) * 4 - 896;
    int ho = (p * 8 + 2 * w) * 2048 + l * 16;
    PIN(ha); PIN(hb); PIN(wsum); PIN(zw); PIN(ylo); PIN(yhi); PIN(ho);
    SB();
    if (colwg) { drain(); STAMP(10); bar(); }
    bar();
    __builtin_amdgcn_s_setprio(2);
    STAMP(11);
    v4u hA[2], hB[2];
    {
        v4u v[2][2];
#pragma unroll
        for (int u = 0; u < 2; ++u) { v[u][0] = __builtin_amdgcn_raw_buffer_load_b128(rE, eoff + u * 256, 0, AUX_SC1); v[u][1] = __builtin_amdgcn_raw_buffer_load_b128(rE, eoff + u * 256 + 2048, 0, AUX_SC1); }
        f4 d[8];
#pragma unroll
        for (int i = 0; i < 8; ++i) d[i] = mm16k16(D8i, v2u{v[i >> 2][0][i & 3], v[i >> 2][1][i & 3]}, zero4);
        d[7][2] += nyq * (d[7][0] - d[7][2]); d[7][3] += nyq * (d[7][1] - d[7][3]);
#pragma unroll
        for (int u = 0; u < 2; ++u) {
            *(LAS v4u*)(sm + zw + u * 256) = v4u{pkh(d[4 * u][0], d[4 * u][1]), pkh(d[4 * u + 1][0], d[4 * u + 1][1]), pkh(d[4 * u + 2][0], d[4 * u + 2][1]), pkh(d[4 * u + 3][0], d[4 * u + 3][1])};
            *(LAS v4u*)(sm + zw + u * 256 + 2048) = v4u{pkh(d[4 * u][2], d[4 * u][3]), pkh(d[4 * u + 1][2], d[4 * u + 1][3]), pkh(d[4 * u + 2][2], d[4 * u + 2][3]), pkh(d[4 * u + 3][2], d[4 * u + 3][3])}; }
        STAMP(12);
        bar();
        v4u z[4];
        { v2u y0, y1, y2, y3, y4, y5, y6, y7;
          asm volatile("ds_read2_b32 %2, %8 offset0:128 offset1:160\n\tds_read2_b32 %3, %8 offset0:192 offset1:224\n\t"
                       "ds_read2_b32 %4, %9 offset0:224 offset1:192\n\tds_read2_b32 %5, %9 offset0:160 offset1:128\n\t"
                       "ds_read2_b32 %6, %9 offset0:96 offset1:64\n\tds_read2_b32 %7, %9 offset0:32 offset1:0\n\t"
                       "ds_read2_b32 %0, %8 offset0:0 offset1:32\n\tds_read2_b32 %1, %8 offset0:64 offset1:96\n\t"
                       "s_waitcnt lgkmcnt(0)"
                       : "=&v"(y0), "=&v"(y1), "=&v"(y2), "=&v"(y3), "=&v"(y4), "=&v"(y5), "=&v"(y6), "=&v"(y7) : "v"(ylo), "v"(yhi) : "memory");
          z[0] = v4u{l == 0 ? 0u : y0[0], y0[1], y1[0], y1[1]}; z[1] = v4u{y2[0], y2[1], y3[0], y3[1]}; z[2] = v4u{y4[0], y4[1], y5[0], y5[1]}; z[3] = v4u{y6[0], y6[1], y7[0], y7[1]}; }
        f16v tr = zero16, ti = zero16;
#pragma unroll
        for (int s = 0; s < 4; ++s) { const int s_ = (s + 1) & 3; tr = mm32h(z[s_], M1[s_], tr); ti = mm32h(z[s_], M2[s_], ti); }
        const f16v ur = tr * twc + ti * tws, ui = ti * twc - tr * tws;
#pragma unroll
        for (int r = 0; r < 16; ++r) z[r >> 2][r & 3] = pkh(ur[r], ui[r]);
#pragma unroll
        for (int s = 0; s < 4; ++s) { hb = mm32h(z[s], M1[s], hb); ha = mm32h(z[s], M2[s], ha); }
#pragma unroll
        for (int r = 0; r < 16; r += 2) { hA[r >> 3][(r >> 1) & 3] = pkb(ha[r], ha[r + 1]); hB[r >> 3][(r >> 1) & 3] = pkb(hb[r], hb[r + 1]); }
    }
    auto rH = RSRC(ws + WS_H, 4 << 20);
    __builtin_amdgcn_raw_buffer_store_b128(hA[0], rH, ho, 0, AUX_SC1); __builtin_amdgcn_raw_buffer_store_b128(hA[1], rH, ho + 1024, 0, AUX_SC1);
    __builtin_amdgcn_raw_buffer_store_b128(hB[0], rH, ho + 2048, 0, AUX_SC1); __builtin_amdgcn_raw_buffer_store_b128(hB[1], rH, ho + 3072, 0, AUX_SC1);
    __builtin_amdgcn_sched_barrier(0);
    STAMP(13);
    float mA, mB, rsA, rsB;
    auto rS = RSRC(ws + WS_ST, 65536);
    {
        float s1a = 0.f, s2a = 0.f, s3a = 0.f, s1b = 0.f, s2b = 0.f, s3b = 0.f;
#pragma unroll
        for (int i = 0; i < 8; ++i) { const u32 a = hA[i >> 2][i & 3], b = hB[i >> 2][i & 3], q = wq[i >> 2][i & 3];
            s1a = dot2(a, 0x3f803f80u, s1a); s2a = dot2(a, a, s2a); s3a = dot2(a, q, s3a); s1b = dot2(b, 0x3f803f80u, s1b); s2b = dot2(b, b, s2b); s3b = dot2(b, q, s3b); }
        const float s3h = swapadd32(s3a, s3b), red3 = rowsum(swapadd16(s3h, s3h));
        float mean, sd; ln_scalars(red4(s1a, s1b, s2a, s2b), mean, sd);
        const float m = rbf(mean), rs = rcpf(sd);
        const u32 rb = pkb((red3 - m * wsum) * rs, 0.f) & 0xffffu;
        const u32 key = (rb ^ ((rb & 0x8000u) ? 0xffffu : 0x8000u));
        if ((l & 31) == 0) {
            *(LAS u16*)(sm + L_SM + 16 + w * 4 + hh * 2) = (u16)key;
            __builtin_amdgcn_raw_buffer_store_b32((f2u(m) >> 16) | f2u(sd), rS, (p * 4 + w) * 64 + hh * 4, 0, AUX_SC1);
        }
        mA = rdl(m, 0); mB = rdl(m, 32); rsA = rdl(rs, 0); rsB = rdl(rs, 32);
    }
    STAMP(14);
    bar();
    drain();
    bar();
    __builtin_amdgcn_s_setprio(0);
    STAMP(15);
    PIN(mA); PIN(mB);
    v4u oA[2], oB[2];
#pragma unroll
    for (int s2 = 0; s2 < 2; ++s2)
#pragma unroll
        for (int d = 0; d < 4; ++d) {
            { const f2 h = {blo(hA[s2][d]), bhi(hA[s2][d])}, c0 = h - mA; const u32 c = pkb(c0[0], c0[1]); const f2 n0 = f2{blo(c), bhi(c)} * rsA; const u32 n = pkb(n0[0], n0[1]); const f2 o = h + f2{blo(n), bhi(n)}; oA[s2][d] = pkb(o[0], o[1]); }
            { const f2 h = {blo(hB[s2][d]), bhi(hB[s2][d])}, c0 = h - mB; const u32 c = pkb(c0[0], c0[1]); const f2 n0 = f2{blo(c), bhi(c)} * rsB; const u32 n = pkb(n0[0], n0[1]); const f2 o = h + f2{blo(n), bhi(n)}; oB[s2][d] = pkb(o[0], o[1]); } }
    {
        LAS char* ob = sm + L_R0 + w * 4352;
#pragma unroll
        for (int s2 = 0; s2 < 2; ++s2) { const int o = 64 * pl + (((2 * s2 + hh) ^ (pl >> 1)) & 3) * 16; *(LAS v4u*)(ob + o) = oA[s2]; *(LAS v4u*)(ob + 2048 + o) = oB[s2]; }
        asm volatile("s_waitcnt lgkmcnt(0)" ::: "memory");
#pragma unroll
        for (int s2 = 0; s2 < 2; ++s2) { const int a_ = 16 * s2 + (l >> 2), o = 64 * a_ + (((l & 3) ^ (a_ >> 1)) & 3) * 16; oA[s2] = *(LAS v4u*)(ob + o); oB[s2] = *(LAS v4u*)(ob + 2048 + o); }
    }
    auto store_out = [&]() {
        const int pos = 2047 - (int)((smw[8 + (l & 7)] >> 5) & 2047u);
        const bool selA = __any(pos == p * 8 + 2 * w), selB = __any(pos == p * 8 + 2 * w + 1);
        auto ro = RSRC(out, S_ * D_ * 2); const int oo = (p + 256 * w) * 2048 + l * 16;
        if (!selA) { __builtin_amdgcn_raw_buffer_store_b128(oA[0], ro, oo, 0, AUX_SC1); __builtin_amdgcn_raw_buffer_store_b128(oA[1], ro, oo + 1024, 0, AUX_SC1); }
        if (!selB) { __builtin_amdgcn_raw_buffer_store_b128(oB[0], ro, oo, 1024 * 2048, AUX_SC1); __builtin_amdgcn_raw_buffer_store_b128(oB[1], ro, oo + 1024, 1024 * 2048, AUX_SC1); } };
    if (colwg) {
        bar();
        STAMP(16);
        down_phase(ws, out, sm, w8, l, grp, rk, store_out STARG);
    } else if (rk < 14) {
        for (u32 sp = 0; sp < SPIN_CAP; ++sp) { if (*(volatile LAS u32*)smw) break; __builtin_amdgcn_s_sleep(1); }
        asm volatile("" ::: "memory");
        auto rA = RSRC(ws + WS_ACT, 8 * 2 * 7 * 1024);
        v4u Bf[8][4];
        { const int sw = ((l15 >> 3) | (((w ^ g4) & 1) << 1)) * 32; LAS char* b = sm + L_W + (32 * g4 + 8 * w + (l15 >> 2)) * 128 + (l15 & 3) * 8;
#pragma unroll
          for (int ct = 0; ct < 4; ++ct) { LAS char* b2 = b + ((ct * 32) ^ sw);
#pragma unroll
            for (int k = 0; k < 8; ++k) { const v2u lo = trd(b2 + swap23(4 * k) * 4096), hi = trd(b2 + swap23(4 * k) * 4096 + 512); Bf[k][ct] = v4u{lo[0], lo[1], hi[0], hi[1]}; } } }
        __builtin_amdgcn_sched_barrier(0);
#pragma unroll
        for (int k = 0; k < 8; ++k) { PIN(Bf[k][0]); PIN(Bf[k][1]); PIN(Bf[k][2]); PIN(Bf[k][3]); }
        const u32 tag = smw[1];
        const int tok = (t >> 2) & 7;
        int xw = L_R0 + w * 4352 + (l >> 3) * 544 + (l & 7) * 16, xrd = (l15 < 8 ? L_R0 + w * 4352 + l15 * 544 : L_TW + w * 512) + g4 * 16;
        if (l < 32) *(LAS v4u*)(sm + L_TW + w * 512 + l * 16) = v4u{0x3f803f80u, 0x3f803f80u, 0x3f803f80u, 0x3f803f80u};
        int pwr = L_W + w * 2448 + l15 * 272 + g4 * 16, pcs = L_W + 8 * 272 + ((t >> 5) * 8 + (t & 3) * 2) * 4, prd = pcs + (tok - 8) * 272;
        PIN(xw); PIN(xrd); PIN(pwr); PIN(pcs); PIN(prd);
        STAMP(16);
        bar();
        STAMP(17);
        const int pos = 2047 - (int)((smw[8 + (l >> 3)] >> 5) & 2047u), ptok = 2047 - (int)((smw[8 + tok] >> 5) & 2047u);
        v4u a[8];
        const int ao = pos * 2048 + w * 512 + (l & 7) * 16;
#pragma unroll
        for (int k = 0; k < 4; ++k) a[k] = __builtin_amdgcn_raw_buffer_load_b128(rH, ao + k * 128, 0, AUX_SC1);
        const u32 st = __builtin_amdgcn_raw_buffer_load_b32(rS, (ptok >> 1) * 64 + (ptok & 1) * 4, 0, AUX_SC1);
#pragma unroll
        for (int k = 0; k < 4; ++k) *(LAS v4u*)(sm + xw + k * 128) = a[k];
        asm volatile("s_waitcnt lgkmcnt(0)" ::: "memory");
#pragma unroll
        for (int k = 0; k < 8; ++k) a[k] = *(LAS v4u*)(sm + xrd + k * 64);
        SB();
        f4 acc[4] = {zero4, zero4, zero4, zero4};
#pragma unroll
        for (int k = 0; k < 8; ++k)
#pragma unroll
            for (int ct = 0; ct < 4; ++ct) acc[ct] = mm16b(Bf[k][ct], a[k], acc[ct]);
        STAMP(18);
        if (l15 < 9) {
#pragma unroll
            for (int ct = 0; ct < 4; ++ct) *(LAS f4*)(sm + pwr + ct * 64) = acc[ct];
        }
        bar();
        f2 sum = {0.f, 0.f}, cs = {0.f, 0.f};
#pragma unroll
        for (int ww = 0; ww < 4; ++ww) { sum += *(LAS f2*)(sm + prd + ww * 2448); cs += *(LAS f2*)(sm + pcs + ww * 2448); }
        const float m = blo(st), rs = rcpf(bhi(st));
        u32 o = pkb((sum[0] - m * cs[0]) * rs, (sum[1] - m * cs[1]) * rs);
        if (!(rk & 1)) { const float g0 = blo(o), g1 = bhi(o); o = pkb(g0 * sigm(g0), g1 * sigm(g1)); }
        __builtin_amdgcn_raw_buffer_store_b32(o, rA, ((grp * 2 + (rk & 1)) * 7 + (rk >> 1)) * 1024 + t * 4, 0, AUX_SC1);
        STAMP(19);
        drain();
        if (l < 4) { GAS u32* fa = (GAS u32*)(ws + WS_FLAG2 + (grp * 4 + l) * 256 + (rk * 4 + w) * 4); asm volatile("global_atomic_swap %0, %1, off sc1" :: "v"(fa), "v"(tag) : "memory"); }
        STAMP(20);
        store_out();
    } else {
        bar();
        store_out();
    }
    STAMP(22);
    STAMP_OUT;
    } else {
    const int rep = (blockIdx.x & 7) | ((blockIdx.x >> 4) & 3) << 3;
    u32 tag = *(const __attribute__((address_space(4))) u32*)(ws + WS_EPOCH) + 1u;
    GAS u64* kslot = (GAS u64*)(ws + WS_KEY + (l & 31) * 8192 + (p >> 3) * 256 + (p & 7) * 8);
    const v4u rFr = mkrsrc(fwr, S_ * DF_ * 2), rFi = mkrsrc(fwi, S_ * DF_ * 2);
    auto init_state = [&]() {
        asm volatile("" : "+s"(tag));
        if (w8 == 4) {
            __builtin_amdgcn_raw_buffer_store_b32(0u, RSRC(ws + WS_ACC, 65536), p * 256 + l * 4, 0, AUX_SC1);
            if (l < 32) { const u64 z = 0; asm volatile("global_atomic_swap_x2 %0, %1, off sc1" :: "v"(kslot), "v"(z) : "memory"); }
            if (l == 0) { smw[0] = 0u; smw[1] = tag; }
        } };
    auto publish_keys = [&]() {
        const v4u kv = *(LAS v4u*)(smw + 4);
        if (l < 4) { GAS u32* a = (GAS u32*)(ws + WS_RK + (p * 4 + l) * 4); const u32 d = smw[4 + l]; asm volatile("global_atomic_swap %0, %1, off sc1" :: "v"(a), "v"(d) : "memory"); }
        u32 a = 0, b = 0, c = 0;
#pragma unroll
        for (int j = 0; j < 8; ++j) { const u32 k = ((j & 1) ? (kv[j >> 1] & 0xffff0000u) : (kv[j >> 1] << 16)) | (u32)((2047 - p * 8 - j) << 5);
            c = med3(b, c, k); b = med3(a, b, k); a = max(a, k); }
        const u32 dl = min((b >> 16) - (c >> 16), 511u);
        const u64 mine = ((u64)(b | (dl & 31u)) << 32) | (a | 16u | (dl >> 5));
        asm volatile("s_mov_b32 exec_hi, 0\n\ts_waitcnt vmcnt(0) lgkmcnt(0)\n\ts_barrier\n\tglobal_atomic_swap_x2 %0, %1, off sc1\n\ts_mov_b64 exec, -1" :: "v"(kslot), "v"(mine) : "memory");
    };
    if (colwg) {
        phase2(ws, sm, w8, l, grp, P0, [&]() {}, [&]() {
            init_state();
            if (w8 == 4) {
                const SyncOps so = sync_ops<2>(ws + WS_F1, p, rep, tag, l);
                bar(); drain();
                sync_arrive(so); sync_wait2<0>(so);
            } else if (w8 == 5) {
                const u32 fo = ((grp + 8 * (l >> 2)) * DF_ + 32 * rk + 1 + (l & 3) * 8) * 2;
                bar();
                __builtin_amdgcn_s_sleep(GDELAY);
#pragma unroll 1
                for (int i = 0; i < WB; ++i) { dma16(rFr, fo, i * 128 * DF_ * 2, L_WF + i * 1056); dma16(rFi, fo, i * 128 * DF_ * 2, L_WF + (16 + i) * 1056); }
                STAMP(1);
                bar();
                __builtin_amdgcn_s_sleep(1);
#pragma unroll 1
                for (int i = WB; i < 16; ++i) { dma16(rFr, fo, i * 128 * DF_ * 2, L_WF + i * 1056); dma16(rFi, fo, i * 128 * DF_ * 2, L_WF + (16 + i) * 1056); }
                drain();
                STAMP(2);
            } else { bar(); drain(); bar(); }
            asm volatile("" :: "v"(cpf));
            bar();
        } STARG);
        if (w8 == 4) {
            const SyncOps so = sync_ops<1>(ws + WS_F2, grp * 16 + rk, rep, tag, l);
            drain();
            sync_arrive(so); sync_wait1<0>(so);
            bar();
            if (blockIdx.x == 0 && l == 0) *(u32*)(ws + WS_EPOCH) = tag;
            bar(); bar();
            publish_keys();
            select8<6>(ws, 28 + (rk & 3), l, smw);
        } else {
            drain();
            Fill f = fill_init(wd + (size_t)grp * 448 * D_ + rk * 64, D_ * 2, 448, l);
            bar(); bar(); bar();
            if (w8 == 5) fill<60>(f, DB8);
            bar(); bar();
            if (w8 == 5) { __builtin_amdgcn_s_sleep(1); fill<DEPTHD>(f, 14 - DB8); drain(); STAMP(3); }
        }
        bar();
        down_phase(ws, out, sm, w8, l, grp, rk, [&]() {} STARG);
    } else if (w8 == 4) {
        init_state();
        { const SyncOps so = sync_ops<2>(ws + WS_F1, p, rep, tag, l);
          bar(); drain();
          asm volatile("" :: "v"(cpf));
          sync_arrive(so); }
        { const SyncOps so = sync_ops<1>(ws + WS_F2, 0, rep, tag, l);
          for (u32 sp = 0; sp < SPIN_CAP; ++sp) { if (*(volatile LAS u32*)(smw + 2) == tag) break; __builtin_amdgcn_s_sleep(4); }
          sync_wait1<0>(so); }
        bar();
        bar(); bar();
        publish_keys();
        STAMP(5);
        if (rk < 14) select8<0>(ws, (grp * 14 + rk) % 28, l, smw); else select8<6>(ws, 28 + (rk & 3), l, smw);
        STAMP(7);
        bar();
    } else if (w8 == 5) {
        { const u32 to = ((grp + 8 * (16 * rk + (l >> 3))) * DF_ + (l & 7) * 64) * 2, to2 = l < 16 ? ((grp + 8 * (16 * rk + l)) * DF_ + 512) * 2 : 0x7ffffff0u;
          __builtin_amdgcn_s_sleep(LDELAY);
          touch(rFr, to, 0, L_JUNK); touch(rFi, to, 0, L_JUNK); touch(rFr, to, 64 * DF_ * 2, L_JUNK); touch(rFi, to, 64 * DF_ * 2, L_JUNK);
          touch(rFr, to2, 0, L_JUNK); touch(rFi, to2, 0, L_JUNK); }
        init_state();
        const u16* wbase = ((rk & 1) ? wu : wg) + grp * 448;
        const int slow = (3 - (int)((u64)wbase >> 7)) & 7;
        Fill f = fill_init(wbase + (rk < 14 ? (rk >> 1) : min(slow, 6)) * 64, F_ * 2, 1024, l);
        const SyncOps so = sync_ops<2>(ws + WS_F1, 0, rep, tag, l);
        const int on = rk < 14 ? 1 : 0;
        auto assist = [&](int i0, int i1) {
            if (rk >= 14 && slow < 7)
#pragma unroll 1
                for (int i = i0; i < i1; ++i) { touch(f.r, l * (F_ * 2), i * 64 * F_ * 2, L_JUNK); asm volatile("s_waitcnt vmcnt(2)" ::: "memory"); }
        };
        if (l == 0) smw[2] = tag - 1u;
        STAMP(1);
        bar(); bar();
        __builtin_amdgcn_s_sleep(1);
        fill<DEPTH>(f, on * (NS1 / 4));
        assist(AS0, 16);
        STAMP(2);
        if (NS1 < 128 && rk < 14 && (rk >> 1) != slow) { sync_wait2<4>(so); __builtin_amdgcn_s_sleep(TS2); }
        STAMP(3);
        fill<DEPTH>(f, on * ((NS2 - NS1) / 4));
        {
          const v4u rD = mkrsrc(wd + (size_t)grp * 448 * D_, 448u * D_ * 2); const u32 to = (4 * rk + (l >> 4)) * (D_ * 2) + (l & 15) * 128;
#pragma unroll 1
          for (int i = 0; i < ((rk >> 1) == slow ? 0 : NDT); ++i) { touch(rD, to, i * 64 * D_ * 2, L_JUNK); asm volatile("s_waitcnt vmcnt(3)" ::: "memory"); } }
        if (l == 0) *(volatile LAS u32*)(smw + 2) = tag;
        STAMP(4);
        bar(); bar(); bar(); bar();
        __builtin_amdgcn_s_sleep(1);
        fill<DEPTH>(f, on * ((128 - NS2) / 4));
        drain();
        asm volatile("" :: "v"(cpf));
        STAMP(8);
        if (l == 0) *(volatile LAS u32*)smw = 1u;
        asm volatile("s_waitcnt lgkmcnt(0)" ::: "memory");
    } else { drain(); asm volatile("" :: "v"(cpf)); return; }
    STAMP_OUT;
    }
}
}

extern "C" void kernel_launch(void* const* d_in, const int* in_sizes, int n_in, void* d_out, int out_size, void* d_ws, size_t ws_size, hipStream_t stream) {
    (void)in_sizes; (void)n_in; (void)out_size; (void)ws_size;
    (void)hipFuncSetAttribute((const void*)fused, hipFuncAttributeMaxDynamicSharedMemorySize, L_TOTAL);
    fused<<<256, 512, L_TOTAL, stream>>>((const u16*)d_in[0], (const u16*)d_in[1], (const u16*)d_in[2], (const u16*)d_in[4],
                                         (const u16*)d_in[5], (const u16*)d_in[6], (const u16*)d_in[7], (u16*)d_out, (char*)d_ws);
}
```

```cpp
#define DIAG 2
#include <hip/hip_runtime.h>
#include <stdint.h>

typedef unsigned short u16;
typedef unsigned u32;
typedef unsigned long long u64;
typedef u32 v4u __attribute__((ext_vector_type(4)));
typedef u32 v2u __attribute__((ext_vector_type(2)));
typedef float f2 __attribute__((ext_vector_type(2)));
typedef float f4 __attribute__((ext_vector_type(4)));
typedef float f16v __attribute__((ext_vector_type(16)));
typedef _Float16 h2 __attribute__((ext_vector_type(2)));
typedef _Float16 h4 __attribute__((ext_vector_type(4)));
typedef _Float16 h8 __attribute__((ext_vector_type(8)));
typedef __bf16 b2 __attribute__((ext_vector_type(2)));
typedef __bf16 b8 __attribute__((ext_vector_type(8)));
typedef short s4 __attribute__((ext_vector_type(4)));
#define DI __device__ __forceinline__
#define GAS __attribute__((address_space(1)))
#define LAS __attribute__((address_space(3)))
#define BC(T, x) __builtin_bit_cast(T, x)

#define LDELAY 12
#define GDELAY 7
#define WB 8
#define NS1 32
#define TS2 16
#define NS2 128
#define DB8 0
#define AS0 7
#define NDT 0
#define DEPTH 28
#define DEPTHD 28
#define STR_(x) #x
#define STR(x) STR_(x)

namespace {
constexpr int S_ = 2048, D_ = 1024, DF_ = 513, F_ = 3584;
constexpr size_t WS_EPOCH = 0;
constexpr size_t WS_F1 = 4096;
constexpr size_t WS_F2 = WS_F1 + 32 * 16 * 256;
constexpr size_t WS_KEY = WS_F2 + 32 * 8 * 256;
constexpr size_t WS_FLAG2 = WS_KEY + 32 * 32 * 256;
constexpr size_t WS_RK = WS_FLAG2 + 8 * 4 * 256;
constexpr size_t WS_ST = WS_RK + 4096;
constexpr size_t WS_ACT = WS_ST + 1024 * 64;
constexpr size_t WS_ACC = WS_ACT + 8 * 2 * 7 * 1024;
constexpr int EC = 16384 + 128;
constexpr size_t WS_E = 10 << 20;
constexpr size_t WS_H = 5 << 20;
constexpr size_t WS_TS = WS_H + (4 << 20);
static_assert(WS_ACC + 65536 <= WS_H, "map");
constexpr int RU = 17408 + 128;
constexpr int L_W = 0;
constexpr int L_R1 = 0;
constexpr int L_WF = 2 * RU;
constexpr int L_R2 = L_WF + 32 * 1056;
constexpr int L_R0 = 131072;
constexpr int L_SM = L_R0 + 17408;
constexpr int L_TW = L_SM + 256;
constexpr int L_JUNK = L_TW + 8192;
constexpr int L_TOTAL = L_JUNK + 256;
static_assert(L_R2 + 2 * RU <= L_R0 && L_TOTAL <= 163840, "lds");
constexpr u32 SPIN_CAP = 1u << 16;
constexpr int CODE_BYTES = 39 * 1024;

DI float u2f(u32 x) { return BC(float, x); }
DI u32 f2u(float x) { return BC(u32, x); }
DI u32 pkh(float a, float b) { f2 v = {a, b}; return BC(u32, __builtin_convertvector(v, h2)); }
DI u32 pkb(float a, float b) { f2 v = {a, b}; return BC(u32, __builtin_convertvector(v, b2)); }
DI u32 rotp(u32 m) { const h2 v = BC(h2, m), k = {(_Float16)-1.f, (_Float16)1.f}; const h2 r = v.yx * k; return BC(u32, r); }
DI u32 rotm(u32 m) { const h2 v = BC(h2, m), k = {(_Float16)1.f, (_Float16)-1.f}; const h2 r = v.yx * k; return BC(u32, r); }
DI float rbf(float a) { return u2f(pkb(a, a) << 16); }
DI float blo(u32 d) { return u2f(d << 16); }
DI float bhi(u32 d) { return u2f(d & 0xffff0000u); }
DI float dot2(u32 a, u32 b, float c) { return __builtin_amdgcn_fdot2_f32_bf16(BC(b2, a), BC(b2, b), c, false); }
DI f16v mm32h(v4u a, v4u b, f16v c) { return __builtin_amdgcn_mfma_f32_32x32x16_f16(BC(h8, a), BC(h8, b), c, 0, 0, 0); }
DI f16v mm32b(v4u a, v4u b, f16v c) { return __builtin_amdgcn_mfma_f32_32x32x16_bf16(BC(b8, a), BC(b8, b), c, 0, 0, 0); }
DI f4 mm16h(v4u a, v4u b, f4 c) { return __builtin_amdgcn_mfma_f32_16x16x32_f16(BC(h8, a), BC(h8, b), c, 0, 0, 0); }
DI f4 mm16b(v4u a, v4u b, f4 c) { return __builtin_amdgcn_mfma_f32_16x16x32_bf16(BC(b8, a), BC(b8, b), c, 0, 0, 0); }
DI f4 mm16k16(v2u a, v2u b, f4 c) { return __builtin_amdgcn_mfma_f32_16x16x16f16(BC(h4, a), BC(h4, b), c, 0, 0, 0); }
DI v2u trd(LAS char* p) { return BC(v2u, __builtin_amdgcn_ds_read_tr16_b64_v4i16((LAS s4*)p)); }
DI void bar() { asm volatile("s_waitcnt lgkmcnt(0)\n\ts_barrier" ::: "memory"); }
DI void drain() { asm volatile("s_waitcnt vmcnt(0)" ::: "memory"); }
DI float sinr(float r) { return __builtin_amdgcn_sinf(r); }
DI float cosr(float r) { return __builtin_amdgcn_cosf(r); }
DI float rcpf(float x) { return __builtin_amdgcn_rcpf(x); }
DI float sigm(float x) { return rcpf(1.f + __builtin_amdgcn_exp2f(-1.44269504f * x)); }
template <int CTRL> DI float dppf(float v) { return u2f((u32)__builtin_amdgcn_update_dpp(0, (int)f2u(v), CTRL, 0xf, 0xf, true)); }
template <int CTRL> DI u32 dppu(u32 v) { return (u32)__builtin_amdgcn_update_dpp(0, (int)v, CTRL, 0xf, 0xf, true); }
DI float rowsum(float v) { v += dppf<0x128>(v); v += dppf<0x124>(v); v += dppf<0x122>(v); v += dppf<0x121>(v); return v; }
DI float swapadd32(float a, float b) { auto r = __builtin_amdgcn_permlane32_swap(f2u(a), f2u(b), false, false); const u32 x = r[0], y = r[1]; return u2f(x) + u2f(y); }
DI float swapadd16(float a, float b) { auto r = __builtin_amdgcn_permlane16_swap(f2u(a), f2u(b), false, false); const u32 x = r[0], y = r[1]; return u2f(x) + u2f(y); }
DI float rdl(float v, int lane) { return u2f((u32)__builtin_amdgcn_readlane((int)f2u(v), lane)); }
DI u32 rdlu(u32 v, int lane) { return (u32)__builtin_amdgcn_readlane((int)v, lane); }
DI float red4(float a, float b, float c, float d) { return rowsum(swapadd16(swapadd32(a, b), swapadd32(c, d))); }
DI u32 med3(u32 a, u32 b, u32 c) { return max(min(a, b), min(max(a, b), c)); }
DI int swap23(int v) { return (v & 0x13) | ((v & 4) << 1) | ((v & 8) >> 1); }
#define RSRC(p, n) __builtin_amdgcn_make_buffer_rsrc((void*)(p), 0, (n), 0x00020000)
#define PIN(x) asm volatile("" : "+v"(x))
#define SB() __builtin_amdgcn_sched_barrier(0)
constexpr int AUX_NT = 0, AUX_SC1 = 16;

#define STAMPS
#define STAMP(i) do { if ((i) == 0 || (i) == DIAG) { __builtin_amdgcn_sched_barrier(0); stamp_[i] = (u32)__builtin_amdgcn_s_memrealtime(); __builtin_amdgcn_sched_barrier(0); } } while (0)
#define STAMP_DECL u32 stamp_[24]; _Pragma("unroll") for (int i_ = 0; i_ < 24; ++i_) stamp_[i_] = 0
#define STAMP_OUT do { if (blockIdx.x == 0 && w8 == 0) { const u32 dt = stamp_[DIAG] - stamp_[0], t1 = (u32)__builtin_amdgcn_s_memrealtime(); while ((u32)__builtin_amdgcn_s_memrealtime() - t1 < 5u * dt) __builtin_amdgcn_s_sleep(1); } } while (0)

DI void ln_scalars(float red, float& mean, float& sd) {
    const float sq = u2f((u32)__builtin_amdgcn_permlane16_swap(f2u(red), f2u(red), false, false)[1]);
    mean = red * (1.f / 1024.f);
    const float var = fmaxf(sq * (1.f / 1024.f) - mean * mean, 0.f);
    sd = rbf(__builtin_amdgcn_sqrtf(rbf(rbf(var) + 1e-5f)));
}

DI v4u mkrsrc(const void* p, u32 n) { const u64 a = (u64)p; return v4u{(u32)a, (u32)(a >> 32) & 0xffffu, n, 0x00020000u}; }
DI void dma16(v4u r, u32 voff, u32 soff, u32 lds) { asm volatile("s_mov_b32 m0, %3\n\ts_nop 0\n\tbuffer_load_dwordx4 %0, %1, %2 offen lds" :: "v"(voff), "s"(r), "s"(soff), "s"(lds) : "memory"); }
DI void touch(v4u r, u32 voff, u32 soff, u32 lds) { asm volatile("s_mov_b32 m0, %3\n\ts_nop 0\n\tbuffer_load_ushort %0, %1, %2 offen lds" :: "v"(voff), "s"(r), "s"(soff), "s"(lds) : "memory"); }

struct SyncOps { GAS u32* a; GAS u64* p0; u64 want, zero; u32 tag; };
template <int NP> DI SyncOps sync_ops(char* base, int id, int rep, u32 tag, int l) {
    SyncOps o; o.a = (GAS u32*)(base + (l & 31) * (NP * 2048) + (id >> 4) * 256 + (id & 15) * 4); o.p0 = (GAS u64*)(base + rep * (NP * 2048) + (l >> 3) * 256 + (l & 7) * 8);
    o.want = ((u64)tag << 32) | tag; o.zero = 0; o.tag = tag;
    PIN(o.a); PIN(o.p0); PIN(o.want); PIN(o.zero); PIN(o.tag);
    return o;
}
DI void sync_arrive(const SyncOps& o) {
    asm volatile("s_mov_b32 exec_hi, 0\n\ts_waitcnt lgkmcnt(0)\n\ts_barrier\n\tglobal_atomic_swap %0, %1, off sc1\n\ts_mov_b64 exec, -1" :: "v"(o.a), "v"(o.tag) : "memory");
}
template <int NAP> DI void sync_wait1(const SyncOps& o) {
    u64 v0; u32 cap = SPIN_CAP;
    asm volatile(".Lp%=:\n\t"
                 "global_atomic_add_x2 %0, %2, %3, off sc0 sc1\n\t"
                 "s_sub_u32 %1, %1, 1\n\ts_waitcnt vmcnt(0)\n\ts_cbranch_scc1 .Ld%=\n\t"
                 "v_cmp_ne_u64 vcc, %0, %4\n\ts_and_b64 exec, exec, vcc\n\ts_cbranch_scc0 .Ld%=\n\ts_sleep %5\n\ts_branch .Lp%=\n"
                 ".Ld%=:\n\ts_mov_b64 exec, -1"
                 : "=&v"(v0), "+s"(cap) : "v"(o.p0), "v"(o.zero), "v"(o.want), "i"(NAP) : "memory", "vcc", "scc");
}
template <int NAP> DI void sync_wait2(const SyncOps& o) {
    u64 v0, v1, m0, m1; u32 cap = SPIN_CAP;
    asm volatile("s_mov_b64 %2, -1\n\ts_mov_b64 %3, -1\n"
                 ".Lp%=:\n\t"
                 "s_mov_b64 exec, %2\n\ts_cbranch_execz .La%=\n\t"
                 "global_atomic_add_x2 %0, %5, %6, off sc0 sc1\n"
                 ".La%=:\n\t"
                 "s_mov_b64 exec, %3\n\ts_cbranch_execz .Lb%=\n\t"
                 "global_atomic_add_x2 %1, %5, %6, off offset:2048 sc0 sc1\n"
                 ".Lb%=:\n\t"
                 "s_mov_b64 exec, -1\n\t"
                 "s_sub_u32 %4, %4, 1\n\t"
                 "s_waitcnt vmcnt(0)\n\t"
                 "s_cbranch_scc1 .Ld%=\n\t"
                 "v_cmp_ne_u64 %2, %0, %7\n\tv_cmp_ne_u64 %3, %1, %7\n\t"
                 "s_or_b64 vcc, %2, %3\n\t"
                 "s_cbranch_scc0 .Ld%=\n\ts_sleep %8\n\ts_branch .Lp%=\n"
                 ".Ld%=:"
                 : "=&v"(v0), "=&v"(v1), "=&s"(m0), "=&s"(m1), "+s"(cap) : "v"(o.p0), "v"(o.zero), "v"(o.want), "i"(NAP) : "memory", "vcc", "scc");
}
struct Fill { v4u r; u32 a, b, so, m0, step; };
DI Fill fill_init(const u16* base, u32 rowbytes, u32 rows, int l) {
    Fill f; f.r = mkrsrc(base, (rows - 1) * rowbytes + 128u); f.a = (l >> 3) * rowbytes + ((l & 7) ^ ((l >> 3) & 2)) * 16; f.b = f.a ^ 64u;
    f.step = 8u * rowbytes; f.so = 0u - f.step; f.m0 = L_W - 1024; return f;
}
template <int DP> DI void fill(Fill& f, int n) {
    if (n > 0)
        asm volatile("s_mov_b32 m0, %3\n"
                 ".Lf%=:\n\t"
                 "s_add_u32 m0, m0, 0x400\n\ts_add_u32 %2, %2, %6\n\tbuffer_load_dwordx4 %0, %5, %2 offen lds\n\t"
                 "s_add_u32 m0, m0, 0x400\n\ts_add_u32 %2, %2, %6\n\tbuffer_load_dwordx4 %1, %5, %2 offen lds\n\t"
                 "s_add_u32 m0, m0, 0x400\n\ts_add_u32 %2, %2, %6\n\tbuffer_load_dwordx4 %0, %5, %2 offen lds\n\t"
                 "s_add_u32 m0, m0, 0x400\n\ts_add_u32 %2, %2, %6\n\tbuffer_load_dwordx4 %1, %5, %2 offen lds\n\t"
                 "s_sub_u32 %4, %4, 1\n\tv_swap_b32 %0, %1\n\ts_waitcnt vmcnt(%7)\n\ts_cmp_lg_u32 %4, 0\n\ts_cbranch_scc1 .Lf%=\n\t"
                 "s_mov_b32 %3, m0"
                 : "+v"(f.a), "+v"(f.b), "+s"(f.so), "+s"(f.m0), "+s"(n) : "s"(f.r), "s"(f.step), "i"(DP) : "memory", "scc");
}

template <int NAP> DI void select8(char* ws, int rep, int l, LAS u32* smw) {
        u64 v0, v1, v2, v3;
        { GAS u64* p0 = (GAS u64*)(ws + WS_KEY + rep * 8192 + 4096 + (l >> 3) * 256 + (l & 7) * 8); const u64 zero = 0;
          u64 m0, m1, m2, m3; u32 cap = SPIN_CAP;
          asm volatile("s_mov_b64 %4, -1\n\ts_mov_b64 %5, -1\n\ts_mov_b64 %6, -1\n\ts_mov_b64 %7, -1\n"
                 ".Lp%=:\n\t"
                 "s_mov_b64 exec, %4\n\ts_cbranch_execz .La%=\n\t"
                 "global_atomic_add_x2 %0, %9, %10, off offset:-4096 sc0 sc1\n"
                 ".La%=:\n\t"
                 "s_mov_b64 exec, %5\n\ts_cbranch_execz .Lb%=\n\t"
                 "global_atomic_add_x2 %1, %9, %10, off offset:-2048 sc0 sc1\n"
                 ".Lb%=:\n\t"
                 "s_mov_b64 exec, %6\n\ts_cbranch_execz .Lc%=\n\t"
                 "global_atomic_add_x2 %2, %9, %10, off sc0 sc1\n"
                 ".Lc%=:\n\t"
                 "s_mov_b64 exec, %7\n\ts_cbranch_execz .Le%=\n\t"
                 "global_atomic_add_x2 %3, %9, %10, off offset:2048 sc0 sc1\n"
                 ".Le%=:\n\t"
                 "s_mov_b64 exec, -1\n\t"
                 "s_sub_u32 %8, %8, 1\n\t"
                 "s_waitcnt vmcnt(0)\n\t"
                 "s_cbranch_scc1 .Ld%=\n\t"
                 "v_cmp_eq_u64 %4, %0, %10\n\tv_cmp_eq_u64 %5, %1, %10\n\tv_cmp_eq_u64 %6, %2, %10\n\tv_cmp_eq_u64 %7, %3, %10\n\t"
                 "s_or_b64 vcc, %4, %5\n\ts_or_b64 vcc, vcc, %6\n\ts_or_b64 vcc, vcc, %7\n\t"
                 "s_cbranch_scc0 .Ld%=\n\ts_sleep %11\n\ts_branch .Lp%=\n"
                 ".Ld%=:"
                 : "=&v"(v0), "=&v"(v1), "=&v"(v2), "=&v"(v3), "=&s"(m0), "=&s"(m1), "=&s"(m2), "=&s"(m3), "+s"(cap) : "v"(p0), "v"(zero), "i"(NAP) : "memory", "vcc", "scc"); }
        {
          const u32 f0 = (u32)v0, f1 = (u32)v1, f2_ = (u32)v2, f3 = (u32)v3, s0 = (u32)(v0 >> 32), s1 = (u32)(v1 >> 32), s2 = (u32)(v2 >> 32), s3 = (u32)(v3 >> 32);
          u32 a = f0, b = s0, c = 0;
#define INS(k) do { c = med3(b, c, k); b = med3(a, b, k); a = max(a, k); } while (0)
          INS(f1); INS(f2_); INS(f3); INS(s1); INS(s2); INS(s3);
          const u32 a16 = a >> 16, b16 = b >> 16;
          u32 gm = max(a16, dppu<0xb1>(a16)); gm = max(gm, dppu<0x4e>(gm)); gm = max(gm, dppu<0x141>(gm));
          const u32 ot = dppu<0x128>(gm), mx = max(gm, ot), mn = min(gm, ot);
          u32 lo;
          { u32 hi, mid, c1, c2; u64 n1;
            asm volatile("s_nop 0\n\t"
                 "v_readlane_b32 %0, %8, 0\n\tv_readlane_b32 %2, %8, 16\n\tv_readlane_b32 %3, %8, 32\n\tv_readlane_b32 %4, %8, 48\n\t"
                 "s_min_u32 %0, %0, %2\n\ts_min_u32 %3, %3, %4\n\tv_readlane_b32 %1, %9, 0\n\tv_readlane_b32 %2, %9, 16\n\ts_min_u32 %0, %0, %3\n\t"
                 "v_readlane_b32 %3, %9, 32\n\tv_readlane_b32 %4, %9, 48\n\t"
                 "s_max_u32 %1, %1, %2\n\ts_max_u32 %3, %3, %4\n\ts_max_u32 %1, %1, %3\n\ts_add_u32 %1, %1, 1\n"
                 ".Lq%=:\n\t"
                 "s_add_u32 %2, %0, %1\n\ts_lshr_b32 %2, %2, 1\n\t"
                 "v_cmp_le_u32 vcc, %2, %6\n\tv_cmp_le_u32 %5, %2, %7\n\t"
                 "s_cmp_eq_u32 %2, %0\n\ts_cbranch_scc1 .Lz%=\n\t"
                 "s_bcnt1_i32_b64 %3, vcc\n\ts_bcnt1_i32_b64 %4, %5\n\ts_add_u32 %3, %3, %4\n\t"
                 "s_cmp_ge_u32 %3, 8\n\ts_cselect_b32 %0, %2, %0\n\ts_cselect_b32 %1, %1, %2\n\t"
                 "s_cmp_lg_u32 %3, 8\n\ts_cbranch_scc1 .Lq%=\n"
                 ".Lz%=:"
                 : "=&s"(lo), "=&s"(hi), "=&s"(mid), "=&s"(c1), "=&s"(c2), "=&s"(n1) : "v"(a16), "v"(b16), "v"(mn), "v"(mx) : "vcc", "scc"); }
          const u64 ga = __ballot(a16 > lo), gb = __ballot(b16 > lo), ea = __ballot(a16 == lo), eb = __ballot(b16 == lo);
          const u32 n0 = __popcll(ga), n1_ = n0 + __popcll(gb), n2 = n1_ + __popcll(ea);
#define MBC(m, base) __builtin_amdgcn_mbcnt_hi((u32)((m) >> 32), __builtin_amdgcn_mbcnt_lo((u32)(m), (base)))
          const u32 ia = a16 > lo ? MBC(ga, 0) : MBC(ea, n1_), ib = b16 > lo ? MBC(gb, n0) : MBC(eb, n2);
          bool amb = (c >> 16) > lo;
          if (__builtin_expect(gb != 0, 0)) {
#define THIRD(f, s) ((((s) >> 16) - ((((f) & 15u) << 5) | ((s) & 31u))) > lo)
              amb = amb || THIRD(f0, s0) || THIRD(f1, s1) || THIRD(f2_, s2) || THIRD(f3, s3); }
          if (__builtin_expect(!__any(amb), 1)) {
              if (a16 >= lo && ia < 8) smw[8 + ia] = a;
              if (b16 >= lo && ib < 8) smw[8 + ib] = b;
          } else {
              auto rk = RSRC(ws + WS_RK, 4096);
              v4u kv[4];
#pragma unroll
              for (int i = 0; i < 4; ++i) kv[i] = __builtin_amdgcn_raw_buffer_load_b128(rk, (i * 64 + l) * 16, 0, AUX_SC1);
              u32 prev = 0xffffffffu;
#pragma unroll 1
              for (int n = 0; n < 8; ++n) { u32 best = 0;
#pragma unroll
                  for (int j = 0; j < 32; ++j) { const u32 d = kv[j >> 3][(j >> 1) & 3], k = ((j & 1) ? (d & 0xffff0000u) : (d << 16)) | (u32)((2047 - ((j >> 3) * 64 + l) * 8 - (j & 7)) << 5);
                      best = max(best, k < prev ? k : 0u); }
                  best = max(best, dppu<0x128>(best)); best = max(best, dppu<0x124>(best)); best = max(best, dppu<0x122>(best)); best = max(best, dppu<0x121>(best));
                  prev = max(max(rdlu(best, 0), rdlu(best, 16)), max(rdlu(best, 32), rdlu(best, 48)));
                  if (l == 0) smw[8 + n] = prev; }
          } }
}

template <typename F0, typename F> DI void phase2(char* ws, LAS char* sm, int w8, int l, int grp, int P0, F0 mid0, F mid
    , u32 (&stamp_)[24]
    ) {
        const int l15 = l & 15, g4 = l >> 4; const f4 zero4 = {0.f, 0.f, 0.f, 0.f};
        auto rE = RSRC(ws + WS_E, 256 * EC);
        v4u A1[2], A4[2], A2[2][2][2], A3[2][2];
        { float jb = (float)(16 * w8 + grp + 128 * l15) * (1.f / 2048.f); PIN(jb);
#pragma unroll
          for (int i = 0; i < 4; ++i) { const float bf = (float)(4 * g4 + i), a0 = bf * jb;
#pragma unroll
            for (int tau = 0; tau < 2; ++tau) { const float a = bf * (float)(tau * (8.f / 2048.f)) + a0, c = cosr(a), s = sinr(a); const u32 m = pkh(c, s), n = pkh(-s, c);
                A2[tau][0][0][i] = m; A2[tau][0][1][i] = n; A2[tau][1][0][i ^ 1] = m; A2[tau][1][1][i ^ 1] = n; } }
          const float c8 = (float)(8 * l15 + grp) * (1.f / 128.f);
#pragma unroll
          for (int i = 0; i < 4; ++i) { const float a = (float)(4 * g4 + i) * c8, c = cosr(a) * 0.125f, s = sinr(a) * 0.125f; A1[0][i] = pkh(c, s); A1[1][i] = pkh(-s, c); }
          PIN(A1[0]); PIN(A1[1]);
#pragma unroll
          for (int tau = 0; tau < 2; ++tau) { PIN(A2[tau][0][0]); PIN(A2[tau][0][1]); PIN(A2[tau][1][0]); PIN(A2[tau][1][1]); }
          SB();
          mid0();
          float bf = (float)l15 * (1.f / 2048.f); PIN(bf); const float a0 = bf * (float)(16 * w8 + grp + 512 * g4);
#pragma unroll
          for (int i = 0; i < 4; ++i)
#pragma unroll
            for (int tau = 0; tau < 2; ++tau) { const float a = bf * (float)(8 * tau + 128 * i) + a0, c = cosr(a), s = sinr(a); A3[tau][0][i] = pkh(c, -s); A3[tau][1][i] = pkh(s, c); }
          const float b8 = bf * 16.f, a8 = b8 * (float)(32 * g4 + grp);
#pragma unroll
          for (int i = 0; i < 4; ++i) { const float a = b8 * (float)(8 * i) + a8, c = cosr(a) * 0.125f, s = sinr(a) * 0.125f; A4[0][i] = pkh(c, -s); A4[1][i] = pkh(s, c); } }
        int goff = (64 * g4 + 2 * w8 + ((l >> 3) & 1)) * EC + P0 * 64 + (l & 7) * 16;
        int a1w0 = L_R1 + (g4 & 1) * RU + l15 * 1040 + ((2 * (w8 & 1) + (g4 >> 1)) ^ (g4 & 1)) * 4 + (w8 >> 1) * 16, a1w1 = a1w0 + (((w8 >> 1) ^ 2) - (w8 >> 1)) * 16;
        int a1r = L_R1 + 2 * w8 * 1040 + l15 * 64 + (g4 ^ ((l15 >> 3) << 1)) * 16;
        int a2w = L_R2 + 4 * g4 * 1088 + l15 * 64 + ((w8 >> 1) ^ ((l15 >> 1) & 3) ^ (l15 >> 3)) * 16 + (w8 & 1) * 8;
        const int tq = l & 3, sl = g4 ^ ((2 * tq) & 3) ^ (tq >> 1);
        int a2r0 = L_R2 + ((l >> 2) & 1) * RU + (2 * w8 + ((l >> 3) & 1)) * 1088 + tq * 256 + sl * 16, a2r1 = a2r0 ^ 16;
        int fb = L_WF + (4 * g4 + (l15 >> 2)) * 1056 + 2 * w8 * 64 + (l15 & 3) * 8;
        PIN(A1[0]); PIN(A1[1]); PIN(A4[0]); PIN(A4[1]);
#pragma unroll
        for (int tau = 0; tau < 2; ++tau) { PIN(A2[tau][0][0]); PIN(A2[tau][0][1]); PIN(A2[tau][1][0]); PIN(A2[tau][1][1]); PIN(A3[tau][0]); PIN(A3[tau][1]); }
        PIN(goff); PIN(a1w0); PIN(a1w1); PIN(a1r); PIN(a2w); PIN(a2r0); PIN(a2r1); PIN(fb);
        SB();
        mid();
        STAMP(6);
        v4u v[4];
#pragma unroll
        for (int i = 0; i < 4; ++i) v[i] = __builtin_amdgcn_raw_buffer_load_b128(rE, goff, i * 16 * EC, AUX_SC1);
        SB();
        f4 fr[4], fi[4];
#pragma unroll
        for (int k = 0; k < 4; ++k) { const v2u a = trd(sm + fb + k * 32), c = trd(sm + fb + k * 32 + 16 * 1056);
            fr[k] = f4{blo(a[0]), bhi(a[0]), blo(a[1]), bhi(a[1])}; fi[k] = f4{blo(c[0]), bhi(c[0]), blo(c[1]), bhi(c[1])}; }
#pragma unroll
        for (int k = 0; k < 4; ++k) { PIN(fr[k]); PIN(fi[k]); }
        SB();
        f4 ar[4], ai[4];
#pragma unroll
        for (int e = 0; e < 4; ++e) { const v4u f = {v[0][e], v[1][e], v[2][e], v[3][e]}; ar[e] = mm16h(f, A1[0], zero4); ai[e] = mm16h(f, A1[1], zero4); }
        SB();
#pragma unroll
        for (int e = 0; e < 4; e += 2)
#pragma unroll
            for (int i = 0; i < 4; ++i) { LAS u32* d = (LAS u32*)(sm + (i < 2 ? a1w0 : a1w1) + (4 * i + e) * 64); d[0] = pkh(ar[e][i], ai[e][i]); d[16] = pkh(ar[e + 1][i], ai[e + 1][i]); }
        STAMP(7);
        bar();
#pragma unroll
        for (int k = 0; k < 4; ++k) v[k] = *(LAS v4u*)(sm + a1r + (k >> 1) * 1040 + (k & 1) * RU);
        SB();
#pragma unroll
        for (int k = 0; k < 4; ++k) { ar[k] = mm16h(A2[k >> 1][k & 1][0], v[k], zero4); ai[k] = mm16h(A2[k >> 1][k & 1][1], v[k], zero4); }
        SB();
        f4 yr[4], yi[4];
#pragma unroll
        for (int k = 0; k < 4; ++k) { yr[k] = ar[k] * fr[k] - ai[k] * fi[k]; yi[k] = ar[k] * fi[k] + ai[k] * fr[k]; }
        SB();
#pragma unroll
        for (int k = 0; k < 4; ++k) { const v4u y = {pkh(yr[k][0], yi[k][0]), pkh(yr[k][1], yi[k][1]), pkh(yr[k][2], yi[k][2]), pkh(yr[k][3], yi[k][3])};
            ar[k] = mm16h(A3[k >> 1][0], y, zero4); ai[k] = mm16h(A3[k >> 1][1], y, zero4); }
        SB();
#pragma unroll
        for (int u = 0; u < 2; ++u)
#pragma unroll
            for (int i = 0; i < 4; ++i) *(LAS v2u*)(sm + a2w + u * RU + i * 1088) = v2u{pkh(ar[u][i], ai[u][i]), pkh(ar[2 + u][i], ai[2 + u][i])};
        bar();
        STAMP(8);
#pragma unroll
        for (int e = 0; e < 4; ++e) v[e] = *(LAS v4u*)(sm + (e < 2 ? a2r0 : a2r1) + e * 64);
        SB();
#pragma unroll
        for (int e = 0; e < 4; ++e) { ar[e] = mm16h(A4[0], v[e], zero4); ai[e] = mm16h(A4[1], v[e], zero4); }
        SB();
#pragma unroll
        for (int i = 0; i < 4; ++i) v[i] = v4u{pkh(ar[0][i], ai[0][i]), pkh(ar[1][i], ai[1][i]), pkh(ar[2][i], ai[2][i]), pkh(ar[3][i], ai[3][i])};
#pragma unroll
        for (int i = 0; i < 4; ++i) __builtin_amdgcn_raw_buffer_store_b128(v[i], rE, goff, i * 16 * EC, AUX_SC1);
        SB();
        STAMP(9);
}
#define STARG , stamp_

template <typename F> DI void down_phase(char* ws, u16* out, LAS char* sm, int w8, int l, int grp, int rk, F mid
    , u32 (&stamp_)[24]
    ) {
        const int l15 = l & 15, g4 = l >> 4, t = w8 * 64 + l; const f4 zero4 = {0.f, 0.f, 0.f, 0.f};
        LAS u32* smw = (LAS u32*)(sm + L_SM); const u32 tag = smw[1];
        auto rH = RSRC(ws + WS_H, 4 << 20); auto rS = RSRC(ws + WS_ST, 65536); auto rA = RSRC(ws + WS_ACT, 8 * 2 * 7 * 1024);
        const u32 ktok = smw[8 + w8]; const int ptok = 2047 - (int)((ktok >> 5) & 2047u);
        const int dcol = rk * 64 + l;
        const u32 hv = __builtin_amdgcn_raw_buffer_load_b16(rH, ptok * 2048 + ((dcol >> 3) & 3) * 512 + swap23(dcol >> 5) * 16 + (dcol & 7) * 2, 0, AUX_SC1);
        const u32 st = __builtin_amdgcn_raw_buffer_load_b32(rS, (ptok >> 1) * 64 + (ptok & 1) * 4, 0, AUX_SC1);
        SB();
        mid();
        SB();
        v4u Bf[2][4];
        { const int sw = ((l15 >> 3) | (((w8 ^ g4) & 1) << 1)) * 32; LAS char* b = sm + L_W + (32 * w8 + 8 * g4 + (l15 >> 2)) * 128 + (l15 & 3) * 8;
#pragma unroll
          for (int ct = 0; ct < 4; ++ct) { LAS char* b2 = b + ((ct * 32) ^ sw);
#pragma unroll
            for (int k = 0; k < 2; ++k) { LAS char* b3 = b2 + ((k == 1 && w8 < 6) ? 32768 : 0); const v2u lo = trd(b3), hi = trd(b3 + 512); Bf[k][ct] = v4u{lo[0], lo[1], hi[0], hi[1]}; } } }
        const u32 kb = ktok & 0xffff0000u; const float rsel = u2f((kb & 0x80000000u) ? (kb & 0x7fff0000u) : (~kb & 0xffff0000u));
        float sg = rbf(sigm(rsel));
        float hf = blo(hv), ln = rbf(rbf(hf - blo(st)) * rcpf(bhi(st)));
        int aoff = (grp * 2 + (l15 >> 3)) * 7168 + w8 * 512 + g4 * 128 + (l & 7) * 16, aoff1 = w8 < 6 ? aoff + 4096 : 0x7ffffff0;
        GAS u64* ap = (GAS u64*)(ws + WS_ACC) + rk * 512 + t;
        GAS u16* op = (GAS u16*)out + ((ptok >> 3) + 256 * (((ptok >> 1) & 3) + 4 * (ptok & 1))) * D_ + dcol;
        int pwr = L_R2 + w8 * 2176 + l15 * 272 + g4 * 16, prd = L_R2 + w8 * 272 + l * 4;
#pragma unroll
        for (int k = 0; k < 2; ++k) { PIN(Bf[k][0]); PIN(Bf[k][1]); PIN(Bf[k][2]); PIN(Bf[k][3]); }
        PIN(sg); PIN(ln); PIN(hf); PIN(aoff); PIN(aoff1); PIN(ap); PIN(op); PIN(pwr); PIN(prd);
        STAMP(17);
        if (w8 == 4) {
            GAS u32* pp = (GAS u32*)(ws + WS_FLAG2 + (grp * 4 + (rk & 3)) * 256 + l * 4);
            u32 v, cap = SPIN_CAP;
            asm volatile("s_lshr_b64 exec, -1, 8\n"
                 ".Lp%=:\n\t"
                 "global_atomic_add %0, %2, %3, off sc0 sc1\n\t"
                 "s_sub_u32 %1, %1, 1\n\t"
                 "s_waitcnt vmcnt(0)\n\t"
                 "s_cbranch_scc1 .Ld%=\n\t"
                 "v_cmp_ne_u32 vcc, %0, %4\n\t"
                 "s_and_b64 exec, exec, vcc\n\t"
                 "s_cbranch_scc1 .Lp%=\n"
                 ".Ld%=:\n\t"
                 "s_mov_b64 exec, -1"
                 : "=&v"(v), "+s"(cap) : "v"(pp), "v"(0u), "v"(tag) : "memory", "vcc", "scc");
        }
        bar();
        STAMP(18);
        v4u a[2];
        a[0] = __builtin_amdgcn_raw_buffer_load_b128(rA, aoff, 0, AUX_SC1);
        a[1] = __builtin_amdgcn_raw_buffer_load_b128(rA, aoff1, 0, AUX_SC1);
        f4 acc[4] = {zero4, zero4, zero4, zero4};
#pragma unroll
        for (int k = 0; k < 2; ++k) { v4u f;
#pragma unroll
            for (int d = 0; d < 4; ++d) { const float lo = blo(a[k][d]), hi = bhi(a[k][d]); f[d] = pkb(lo * dppf<0x128>(lo), hi * dppf<0x128>(hi)); }
#pragma unroll
            for (int ct = 0; ct < 4; ++ct) acc[ct] = mm16b(Bf[k][ct], f, acc[ct]); }
        STAMP(19);
        if (l15 < 8) {
#pragma unroll
            for (int ct = 0; ct < 4; ++ct) *(LAS f4*)(sm + pwr + ct * 64) = acc[ct];
        }
        bar();
        float ps[8];
#pragma unroll
        for (int ww = 0; ww < 8; ++ww) ps[ww] = *(LAS float*)(sm + prd + ww * 2176);
        const float sum = ((ps[0] + ps[1]) + (ps[2] + ps[3])) + ((ps[4] + ps[5]) + (ps[6] + ps[7]));
        const float lim = 4.0e9f; const double magic = 103079215104.0;
        const u64 i0 = BC(u64, (double)__builtin_amdgcn_fmed3f(sum, -lim, lim) + magic) + ((1ull << 60) + (1ull << 48) - BC(u64, magic));
        const u64 j0 = i0 + (BC(u64, magic) - (8ull << 60) - (8ull << 48));
        u64 o0;
        STAMP(20);
        asm volatile("global_atomic_add_x2 %0, %1, %2, off sc0 sc1\n\ts_waitcnt vmcnt(0)" : "=&v"(o0) : "v"(ap), "v"(i0) : "memory");
        const float y = rbf(rbf((float)(BC(double, o0 + j0) - magic)) * sg);
        if ((u32)(o0 >> 60) == 7) op[0] = (u16)pkb(hf + rbf(ln + y), 0.f);
}

__global__ void __launch_bounds__(512) fused(const u16* __restrict__ x, const u16* __restrict__ fwr, const u16* __restrict__ fwi, const u16* __restrict__ wrt,
                                             const u16* __restrict__ wg, const u16* __restrict__ wu, const u16* __restrict__ wd, u16* __restrict__ out, char* __restrict__ ws) {
    extern __shared__ __attribute__((aligned(16))) char smem[];
    const int t = threadIdx.x, w8 = __builtin_amdgcn_readfirstlane(t >> 6), l = t & 63;
    const int grp = blockIdx.x & 7, rk = blockIdx.x >> 4;
    const bool colwg = (blockIdx.x & 8) == 0;
    const int p = grp * 32 + (blockIdx.x >> 3), P0 = grp * 32 + 2 * rk;
    LAS char* sm = (LAS char*)smem;
    LAS u32* smw = (LAS u32*)(sm + L_SM);
    STAMP_DECL;
    STAMP(0);
    u32 cpf;
    { const int o = min((int)((blockIdx.x >> 3) + (t & 15) * 32) * 128, CODE_BYTES - 128);
      asm volatile("s_getpc_b64 vcc\n\ts_mov_b64 exec, 0x3ff\n\tglobal_load_dword %0, %1, vcc\n\ts_mov_b64 exec, -1" : "=&v"(cpf) : "v"(o) : "vcc"); }
    asm volatile("" :: "s"(x));

    if (__builtin_expect(w8 < 4, 1)) {
    const int w = w8, l15 = l & 15, g4 = l >> 4;
    const f4 zero4 = {0.f, 0.f, 0.f, 0.f};
    auto rE = RSRC(ws + WS_E, 256 * EC);
    __builtin_amdgcn_s_setprio(2);
    const int hh = l >> 5, pl = swap23(l & 31);
    auto rx = RSRC(x, S_ * D_ * 2);
    v4u xr[2][2];
    { const int vo = (p + 256 * w) * 2048 + 64 * pl + 16 * hh;
      xr[0][0] = __builtin_amdgcn_raw_buffer_load_b128(rx, vo, 0, AUX_NT);
      xr[1][0] = __builtin_amdgcn_raw_buffer_load_b128(rx, vo, 1024 * 2048, AUX_NT);
      xr[0][1] = __builtin_amdgcn_raw_buffer_load_b128(rx, vo + 32, 0, AUX_NT);
      xr[1][1] = __builtin_amdgcn_raw_buffer_load_b128(rx, vo + 32, 1024 * 2048, AUX_NT); }
    SB();
    v4u wq[2];
    { auto rw = RSRC(wrt, 2048); const int o = 64 * pl + 16 * hh; wq[0] = __builtin_amdgcn_raw_buffer_load_b128(rw, o, 0, 0); wq[1] = __builtin_amdgcn_raw_buffer_load_b128(rw, o + 32, 0, 0); }
    __builtin_amdgcn_sched_barrier(0);
    STAMP(21);

    v4u M1[4], M2[4]; f16v twc, tws;
    { const float plf = (float)pl * (1.f / 1024.f), b0 = plf * (float)(8 * hh), pl32 = plf * 32.f, b32 = b0 * 32.f;
      const u32 sgn = (pl & 1) ? 0x80008000u : 0u;
#pragma unroll
      for (int r = 0; r < 8; ++r) {
        const float a32 = pl32 * (float)r + b32, c = cosr(a32), s = sinr(a32);
        const u32 m1 = pkh(c, s), m2 = pkh(-s, c);
        M1[r >> 2][r & 3] = m1; M2[r >> 2][r & 3] = m2; M1[2 + (r >> 2)][r & 3] = m1 ^ sgn; M2[2 + (r >> 2)][r & 3] = m2 ^ sgn;
      }
#pragma unroll
      for (int r = 0; r < 16; ++r) { const float a1k = plf * (float)(16 * (r >> 3) + (r & 7)) + b0; twc[r] = cosr(a1k); tws[r] = sinr(a1k); }
      twc *= (1.f / 32.f); tws *= (1.f / 32.f); }
    v4u PI[2];
    { const int k = pl - 8 * hh; const u32 one = (k & 1) ? 0x3f800000u : 0x3f80u;
#pragma unroll
      for (int d = 0; d < 8; ++d) PI[d >> 2][d & 3] = (k >> 1) == 8 * (d >> 2) + (d & 3) ? one : 0u; }
    v2u D8f;
    {
      const float k = 0.35355339f;
      { const int q = l15 >> 1; const float e8 = (q & 1) ? -0.125f : 0.125f, th = (float)(q * g4) * 0.125f + ((l15 & 1) ? 0.25f : 0.f), es = (q & 1) ? -k : k;
        D8f[0] = pkh(k * cosr(th + e8), es * cosr(th - e8)); D8f[1] = pkh(k * cosr(th - e8), es * cosr(th + e8)); }
    }
#pragma unroll
    for (int i = 0; i < 4; ++i) { PIN(M1[i]); PIN(M2[i]); }
    PIN(twc); PIN(tws); PIN(PI[0]); PIN(PI[1]); PIN(D8f);
    if (w == 0) {
#pragma unroll
        for (int i = 0; i < 4; ++i) { *(LAS f4*)(sm + L_TW + i * 2048 + l * 16) = f4{twc[4 * i], twc[4 * i + 1], twc[4 * i + 2], twc[4 * i + 3]};
                                      *(LAS f4*)(sm + L_TW + i * 2048 + 1024 + l * 16) = f4{tws[4 * i], tws[4 * i + 1], tws[4 * i + 2], tws[4 * i + 3]}; }
    }
    f16v zero16;
#pragma unroll
    for (int i = 0; i < 16; ++i) zero16[i] = 0.f;
    int eoff = p * EC + g4 * 4096 + (128 * w + 4 * l15) * 4;
    int zlo = L_R0 + w * 4112 + (pl + 256 * hh - 1) * 8, zhi = L_R0 + w * 4112 + (511 - pl - 256 * hh) * 8 + 4 - 1792, zh0 = L_R0 + w * 4112 + (((pl + 256 * hh - 1) * 8) & 4095);
    int zrd = L_R0 + g4 * 4112 + (128 * w + 4 * l15) * 8;
    PIN(eoff); PIN(zlo); PIN(zhi); PIN(zh0); PIN(zrd);
    __builtin_amdgcn_sched_barrier(0);
    STAMP(1);

    {
        float s1a = 0.f, s2a = 0.f, s1b = 0.f, s2b = 0.f;
#pragma unroll
        for (int i = 0; i < 8; ++i) { const u32 a = xr[0][i >> 2][i & 3], b = xr[1][i >> 2][i & 3];
            s1a = dot2(a, 0x3f803f80u, s1a); s2a = dot2(a, a, s2a); s1b = dot2(b, 0x3f803f80u, s1b); s2b = dot2(b, b, s2b); }
        asm volatile("" :: "v"(cpf), "v"(s1a), "v"(s2b));
        STAMP(2);
        f16v xa = mm32b(xr[0][0], PI[0], zero16); f16v xb = mm32b(xr[1][0], PI[0], zero16);
        xa = mm32b(xr[0][1], PI[1], xa); xb = mm32b(xr[1][1], PI[1], xb);
        float mean, sd; ln_scalars(red4(s1a, s1b, s2a, s2b), mean, sd);
        const float rs = rcpf(sd), of = -mean * rs;
        const float ra = rdl(rs, 0), rb = rdl(rs, 32), oa = rdl(of, 0), ob = rdl(of, 32);
        xa = xa * ra + oa; xb = xb * rb + ob;
        v4u z[4];
#pragma unroll
        for (int r = 0; r < 16; ++r) z[r >> 2][r & 3] = pkh(xa[r], xb[r]);
        f16v tr = zero16, ti = zero16;
#pragma unroll
        for (int s = 0; s < 4; ++s) { tr = mm32h(z[s], M1[s], tr); ti = mm32h(z[s], M2[s], ti); }
        const f16v ur = tr * twc + ti * tws, ui = ti * twc - tr * tws;
#pragma unroll
        for (int r = 0; r < 16; ++r) z[r >> 2][r & 3] = pkh(ur[r], ui[r]);
        tr = zero16; ti = zero16;
#pragma unroll
        for (int s = 0; s < 4; ++s) { tr = mm32h(M1[s], z[s], tr); ti = mm32h(M2[s], z[s], ti); }
        u32 zz[16];
#pragma unroll
        for (int r = 0; r < 16; ++r) zz[r] = pkh(tr[r], ti[r]);
        if (l == 0) zz[0] = zz[8];
        *(LAS u32*)(sm + zh0) = zz[0];
#pragma unroll
        for (int r = 1; r < 8; ++r) *(LAS u32*)(sm + zlo + 256 * r) = zz[r];
#pragma unroll
        for (int r = 0; r < 8; ++r) *(LAS u32*)(sm + zhi + 1792 - 256 * r) = zz[8 + r];
        STAMP(23);
        bar();
        STAMP(3);
        v4u q[4];
#pragma unroll
        for (int i = 0; i < 4; ++i) q[i] = *(LAS v4u*)(sm + zrd + (i >> 1) * 512 + (i & 1) * 16);
        SB();
        f4 d[8];
#pragma unroll
        for (int i = 0; i < 8; ++i) d[i] = mm16k16(D8f, v2u{q[i >> 1][2 * (i & 1)], q[i >> 1][2 * (i & 1) + 1]}, zero4);
#pragma unroll
        for (int u = 0; u < 2; ++u) {
            const v4u o0 = {pkh(d[4 * u][0], d[4 * u][1]), pkh(d[4 * u + 1][0], d[4 * u + 1][1]), pkh(d[4 * u + 2][0], d[4 * u + 2][1]), pkh(d[4 * u + 3][0], d[4 * u + 3][1])};
            const v4u o1 = {pkh(d[4 * u][2], d[4 * u][3]), pkh(d[4 * u + 1][2], d[4 * u + 1][3]), pkh(d[4 * u + 2][2], d[4 * u + 2][3]), pkh(d[4 * u + 3][2], d[4 * u + 3][3])};
            __builtin_amdgcn_raw_buffer_store_b128(o0, rE, eoff + u * 256, 0, AUX_SC1);
            __builtin_amdgcn_raw_buffer_store_b128(o1, rE, eoff + u * 256 + 2048, 0, AUX_SC1);
        }
        __builtin_amdgcn_sched_barrier(0);
        STAMP(4);
    }
    __builtin_amdgcn_s_setprio(0);
    if (colwg) phase2(ws, sm, w8, l, grp, P0, [&]() { drain(); STAMP(5); bar(); }, [&]() { bar(); } STARG);
    else { drain(); STAMP(5); bar(); }
    f16v ha, hb;
#pragma unroll
    for (int r = 0; r < 16; r += 2) { const u32 a = xr[0][r >> 3][(r >> 1) & 3], b = xr[1][r >> 3][(r >> 1) & 3]; ha[r] = blo(a); ha[r + 1] = bhi(a); hb[r] = blo(b); hb[r + 1] = bhi(b); }
    float wsum;
    { float s = 0.f;
#pragma unroll
      for (int i = 0; i < 8; ++i) s = dot2(wq[i >> 2][i & 3], 0x3f803f80u, s);
      wsum = red4(s, s, s, s); }
    v2u D8i;
#pragma unroll
    for (int x2 = 0; x2 < 2; ++x2) { const int q = 2 * g4 + x2, j = (l15 & 3) ^ 1; const float e8 = x2 ? -0.125f : 0.125f, kk = 0.35355339f * 0.25f;
        const float th = (float)(q * (l15 >> 2)) * 0.125f + ((j == 0 || j == 3) ? e8 : -e8), sc = (x2 && (j & 1)) ? -kk : kk;
        D8i[x2] = pkh(sc * cosr(th), sc * cosr(th + 0.25f)); }
#pragma unroll
    for (int i = 0; i < 4; ++i) { const f4 c = *(LAS f4*)(sm + L_TW + i * 2048 + l * 16), s = *(LAS f4*)(sm + L_TW + i * 2048 + 1024 + l * 16);
#pragma unroll
        for (int j = 0; j < 4; ++j) { twc[4 * i + j] = c[j]; tws[4 * i + j] = s[j]; } }
    PIN(D8i); PIN(twc); PIN(tws);
    float nyq = (w == 3 && l15 == 15) ? 0.5f : 0.f;
    PIN(nyq);
    int zw = L_R0 + g4 * 4096 + (128 * w + 4 * l15) * 4;
    int ylo = L_R0 + w * 4096 + (pl + 256 * hh - 1) * 4, yhi = L_R0 + w * 4096 + 2048 + (511 - pl - 256 * hh) * 4 - 896;
    int ho = (p * 8 + 2 * w) * 2048 + l * 16;
    PIN(ha); PIN(hb); PIN(wsum); PIN(zw); PIN(ylo); PIN(yhi); PIN(ho);
    SB();
    if (colwg) { drain(); STAMP(10); bar(); }
    bar();
    __builtin_amdgcn_s_setprio(2);
    STAMP(11);
    v4u hA[2], hB[2];
    {
        v4u v[2][2];
#pragma unroll
        for (int u = 0; u < 2; ++u) { v[u][0] = __builtin_amdgcn_raw_buffer_load_b128(rE, eoff + u * 256, 0, AUX_SC1); v[u][1] = __builtin_amdgcn_raw_buffer_load_b128(rE, eoff + u * 256 + 2048, 0, AUX_SC1); }
        f4 d[8];
#pragma unroll
        for (int i = 0; i < 8; ++i) d[i] = mm16k16(D8i, v2u{v[i >> 2][0][i & 3], v[i >> 2][1][i & 3]}, zero4);
        d[7][2] += nyq * (d[7][0] - d[7][2]); d[7][3] += nyq * (d[7][1] - d[7][3]);
#pragma unroll
        for (int u = 0; u < 2; ++u) {
            *(LAS v4u*)(sm + zw + u * 256) = v4u{pkh(d[4 * u][0], d[4 * u][1]), pkh(d[4 * u + 1][0], d[4 * u + 1][1]), pkh(d[4 * u + 2][0], d[4 * u + 2][1]), pkh(d[4 * u + 3][0], d[4 * u + 3][1])};
            *(LAS v4u*)(sm + zw + u * 256 + 2048) = v4u{pkh(d[4 * u][2], d[4 * u][3]), pkh(d[4 * u + 1][2], d[4 * u + 1][3]), pkh(d[4 * u + 2][2], d[4 * u + 2][3]), pkh(d[4 * u + 3][2], d[4 * u + 3][3])}; }
        STAMP(12);
        bar();
        v4u z[4];
        { v2u y0, y1, y2, y3, y4, y5, y6, y7;
          asm volatile("ds_read2_b32 %2, %8 offset0:128 offset1:160\n\tds_read2_b32 %3, %8 offset0:192 offset1:224\n\t"
                       "ds_read2_b32 %4, %9 offset0:224 offset1:192\n\tds_read2_b32 %5, %9 offset0:160 offset1:128\n\t"
                       "ds_read2_b32 %6, %9 offset0:96 offset1:64\n\tds_read2_b32 %7, %9 offset0:32 offset1:0\n\t"
                       "ds_read2_b32 %0, %8 offset0:0 offset1:32\n\tds_read2_b32 %1, %8 offset0:64 offset1:96\n\t"
                       "s_waitcnt lgkmcnt(0)"
                       : "=&v"(y0), "=&v"(y1), "=&v"(y2), "=&v"(y3), "=&v"(y4), "=&v"(y5), "=&v"(y6), "=&v"(y7) : "v"(ylo), "v"(yhi) : "memory");
          z[0] = v4u{l == 0 ? 0u : y0[0], y0[1], y1[0], y1[1]}; z[1] = v4u{y2[0], y2[1], y3[0], y3[1]}; z[2] = v4u{y4[0], y4[1], y5[0], y5[1]}; z[3] = v4u{y6[0], y6[1], y7[0], y7[1]}; }
        f16v tr = zero16, ti = zero16;
#pragma unroll
        for (int s = 0; s < 4; ++s) { const int s_ = (s + 1) & 3; tr = mm32h(z[s_], M1[s_], tr); ti = mm32h(z[s_], M2[s_], ti); }
        const f16v ur = tr * twc + ti * tws, ui = ti * twc - tr * tws;
#pragma unroll
        for (int r = 0; r < 16; ++r) z[r >> 2][r & 3] = pkh(ur[r], ui[r]);
#pragma unroll
        for (int s = 0; s < 4; ++s) { hb = mm32h(z[s], M1[s], hb); ha = mm32h(z[s], M2[s], ha); }
#pragma unroll
        for (int r = 0; r < 16; r += 2) { hA[r >> 3][(r >> 1) & 3] = pkb(ha[r], ha[r + 1]); hB[r >> 3][(r >> 1) & 3] = pkb(hb[r], hb[r + 1]); }
    }
    auto rH = RSRC(ws + WS_H, 4 << 20);
    __builtin_amdgcn_raw_buffer_store_b128(hA[0], rH, ho, 0, AUX_SC1); __builtin_amdgcn_raw_buffer_store_b128(hA[1], rH, ho + 1024, 0, AUX_SC1);
    __builtin_amdgcn_raw_buffer_store_b128(hB[0], rH, ho + 2048, 0, AUX_SC1); __builtin_amdgcn_raw_buffer_store_b128(hB[1], rH, ho + 3072, 0, AUX_SC1);
    __builtin_amdgcn_sched_barrier(0);
    STAMP(13);
    float mA, mB, rsA, rsB;
    auto rS = RSRC(ws + WS_ST, 65536);
    {
        float s1a = 0.f, s2a = 0.f, s3a = 0.f, s1b = 0.f, s2b = 0.f, s3b = 0.f;
#pragma unroll
        for (int i = 0; i < 8; ++i) { const u32 a = hA[i >> 2][i & 3], b = hB[i >> 2][i & 3], q = wq[i >> 2][i & 3];
            s1a = dot2(a, 0x3f803f80u, s1a); s2a = dot2(a, a, s2a); s3a = dot2(a, q, s3a); s1b = dot2(b, 0x3f803f80u, s1b); s2b = dot2(b, b, s2b); s3b = dot2(b, q, s3b); }
        const float s3h = swapadd32(s3a, s3b), red3 = rowsum(swapadd16(s3h, s3h));
        float mean, sd; ln_scalars(red4(s1a, s1b, s2a, s2b), mean, sd);
        const float m = rbf(mean), rs = rcpf(sd);
        const u32 rb = pkb((red3 - m * wsum) * rs, 0.f) & 0xffffu;
        const u32 key = (rb ^ ((rb & 0x8000u) ? 0xffffu : 0x8000u));
        if ((l & 31) == 0) {
            *(LAS u16*)(sm + L_SM + 16 + w * 4 + hh * 2) = (u16)key;
            __builtin_amdgcn_raw_buffer_store_b32((f2u(m) >> 16) | f2u(sd), rS, (p * 4 + w) * 64 + hh * 4, 0, AUX_SC1);
        }
        mA = rdl(m, 0); mB = rdl(m, 32); rsA = rdl(rs, 0); rsB = rdl(rs, 32);
    }
    STAMP(14);
    bar();
    drain();
    bar();
    __builtin_amdgcn_s_setprio(0);
    STAMP(15);
    PIN(mA); PIN(mB);
    v4u oA[2], oB[2];
#pragma unroll
    for (int s2 = 0; s2 < 2; ++s2)
#pragma unroll
        for (int d = 0; d < 4; ++d) {
            { const f2 h = {blo(hA[s2][d]), bhi(hA[s2][d])}, c0 = h - mA; const u32 c = pkb(c0[0], c0[1]); const f2 n0 = f2{blo(c), bhi(c)} * rsA; const u32 n = pkb(n0[0], n0[1]); const f2 o = h + f2{blo(n), bhi(n)}; oA[s2][d] = pkb(o[0], o[1]); }
            { const f2 h = {blo(hB[s2][d]), bhi(hB[s2][d])}, c0 = h - mB; const u32 c = pkb(c0[0], c0[1]); const f2 n0 = f2{blo(c), bhi(c)} * rsB; const u32 n = pkb(n0[0], n0[1]); const f2 o = h + f2{blo(n), bhi(n)}; oB[s2][d] = pkb(o[0], o[1]); } }
    {
        LAS char* ob = sm + L_R0 + w * 4352;
#pragma unroll
        for (int s2 = 0; s2 < 2; ++s2) { const int o = 64 * pl + (((2 * s2 + hh) ^ (pl >> 1)) & 3) * 16; *(LAS v4u*)(ob + o) = oA[s2]; *(LAS v4u*)(ob + 2048 + o) = oB[s2]; }
        asm volatile("s_waitcnt lgkmcnt(0)" ::: "memory");
#pragma unroll
        for (int s2 = 0; s2 < 2; ++s2) { const int a_ = 16 * s2 + (l >> 2), o = 64 * a_ + (((l & 3) ^ (a_ >> 1)) & 3) * 16; oA[s2] = *(LAS v4u*)(ob + o); oB[s2] = *(LAS v4u*)(ob + 2048 + o); }
    }
    auto store_out = [&]() {
        const int pos = 2047 - (int)((smw[8 + (l & 7)] >> 5) & 2047u);
        const bool selA = __any(pos == p * 8 + 2 * w), selB = __any(pos == p * 8 + 2 * w + 1);
        auto ro = RSRC(out, S_ * D_ * 2); const int oo = (p + 256 * w) * 2048 + l * 16;
        if (!selA) { __builtin_amdgcn_raw_buffer_store_b128(oA[0], ro, oo, 0, AUX_SC1); __builtin_amdgcn_raw_buffer_store_b128(oA[1], ro, oo + 1024, 0, AUX_SC1); }
        if (!selB) { __builtin_amdgcn_raw_buffer_store_b128(oB[0], ro, oo, 1024 * 2048, AUX_SC1); __builtin_amdgcn_raw_buffer_store_b128(oB[1], ro, oo + 1024, 1024 * 2048, AUX_SC1); } };
    if (colwg) {
        bar();
        STAMP(16);
        down_phase(ws, out, sm, w8, l, grp, rk, store_out STARG);
    } else if (rk < 14) {
        for (u32 sp = 0; sp < SPIN_CAP; ++sp) { if (*(volatile LAS u32*)smw) break; __builtin_amdgcn_s_sleep(1); }
        asm volatile("" ::: "memory");
        auto rA = RSRC(ws + WS_ACT, 8 * 2 * 7 * 1024);
        v4u Bf[8][4];
        { const int sw = ((l15 >> 3) | (((w ^ g4) & 1) << 1)) * 32; LAS char* b = sm + L_W + (32 * g4 + 8 * w + (l15 >> 2)) * 128 + (l15 & 3) * 8;
#pragma unroll
          for (int ct = 0; ct < 4; ++ct) { LAS char* b2 = b + ((ct * 32) ^ sw);
#pragma unroll
            for (int k = 0; k < 8; ++k) { const v2u lo = trd(b2 + swap23(4 * k) * 4096), hi = trd(b2 + swap23(4 * k) * 4096 + 512); Bf[k][ct] = v4u{lo[0], lo[1], hi[0], hi[1]}; } } }
        __builtin_amdgcn_sched_barrier(0);
#pragma unroll
        for (int k = 0; k < 8; ++k) { PIN(Bf[k][0]); PIN(Bf[k][1]); PIN(Bf[k][2]); PIN(Bf[k][3]); }
        const u32 tag = smw[1];
        const int tok = (t >> 2) & 7;
        int xw = L_R0 + w * 4352 + (l >> 3) * 544 + (l & 7) * 16, xrd = (l15 < 8 ? L_R0 + w * 4352 + l15 * 544 : L_TW + w * 512) + g4 * 16;
        if (l < 32) *(LAS v4u*)(sm + L_TW + w * 512 + l * 16) = v4u{0x3f803f80u, 0x3f803f80u, 0x3f803f80u, 0x3f803f80u};
        int pwr = L_W + w * 2448 + l15 * 272 + g4 * 16, pcs = L_W + 8 * 272 + ((t >> 5) * 8 + (t & 3) * 2) * 4, prd = pcs + (tok - 8) * 272;
        PIN(xw); PIN(xrd); PIN(pwr); PIN(pcs); PIN(prd);
        STAMP(16);
        bar();
        STAMP(17);
        const int pos = 2047 - (int)((smw[8 + (l >> 3)] >> 5) & 2047u), ptok = 2047 - (int)((smw[8 + tok] >> 5) & 2047u);
        v4u a[8];
        const int ao = pos * 2048 + w * 512 + (l & 7) * 16;
#pragma unroll
        for (int k = 0; k < 4; ++k) a[k] = __builtin_amdgcn_raw_buffer_load_b128(rH, ao + k * 128, 0, AUX_SC1);
        const u32 st = __builtin_amdgcn_raw_buffer_load_b32(rS, (ptok >> 1) * 64 + (ptok & 1) * 4, 0, AUX_SC1);
#pragma unroll
        for (int k = 0; k < 4; ++k) *(LAS v4u*)(sm + xw + k * 128) = a[k];
        asm volatile("s_waitcnt lgkmcnt(0)" ::: "memory");
#pragma unroll
        for (int k = 0; k < 8; ++k) a[k] = *(LAS v4u*)(sm + xrd + k * 64);
        SB();
        f4 acc[4] = {zero4, zero4, zero4, zero4};
#pragma unroll
        for (int k = 0; k < 8; ++k)
#pragma unroll
            for (int ct = 0; ct < 4; ++ct) acc[ct] = mm16b(Bf[k][ct], a[k], acc[ct]);
        STAMP(18);
        if (l15 < 9) {
#pragma unroll
            for (int ct = 0; ct < 4; ++ct) *(LAS f4*)(sm + pwr + ct * 64) = acc[ct];
        }
        bar();
        f2 sum = {0.f, 0.f}, cs = {0.f, 0.f};
#pragma unroll
        for (int ww = 0; ww < 4; ++ww) { sum += *(LAS f2*)(sm + prd + ww * 2448); cs += *(LAS f2*)(sm + pcs + ww * 2448); }
        const float m = blo(st), rs = rcpf(bhi(st));
        u32 o = pkb((sum[0] - m * cs[0]) * rs, (sum[1] - m * cs[1]) * rs);
        if (!(rk & 1)) { const float g0 = blo(o), g1 = bhi(o); o = pkb(g0 * sigm(g0), g1 * sigm(g1)); }
        __builtin_amdgcn_raw_buffer_store_b32(o, rA, ((grp * 2 + (rk & 1)) * 7 + (rk >> 1)) * 1024 + t * 4, 0, AUX_SC1);
        STAMP(19);
        drain();
        if (l < 4) { GAS u32* fa = (GAS u32*)(ws + WS_FLAG2 + (grp * 4 + l) * 256 + (rk * 4 + w) * 4); asm volatile("global_atomic_swap %0, %1, off sc1" :: "v"(fa), "v"(tag) : "memory"); }
        STAMP(20);
        store_out();
    } else {
        bar();
        store_out();
    }
    STAMP(22);
    STAMP_OUT;
    } else {
    const int rep = (blockIdx.x & 7) | ((blockIdx.x >> 4) & 3) << 3;
    u32 tag = *(const __attribute__((address_space(4))) u32*)(ws + WS_EPOCH) + 1u;
    GAS u64* kslot = (GAS u64*)(ws + WS_KEY + (l & 31) * 8192 + (p >> 3) * 256 + (p & 7) * 8);
    const v4u rFr = mkrsrc(fwr, S_ * DF_ * 2), rFi = mkrsrc(fwi, S_ * DF_ * 2);
    auto init_state = [&]() {
        asm volatile("" : "+s"(tag));
        if (w8 == 4) {
            __builtin_amdgcn_raw_buffer_store_b32(0u, RSRC(ws + WS_ACC, 65536), p * 256 + l * 4, 0, AUX_SC1);
            if (l < 32) { const u64 z = 0; asm volatile("global_atomic_swap_x2 %0, %1, off sc1" :: "v"(kslot), "v"(z) : "memory"); }
            if (l == 0) { smw[0] = 0u; smw[1] = tag; }
        } };
    auto publish_keys = [&]() {
        const v4u kv = *(LAS v4u*)(smw + 4);
        if (l < 4) { GAS u32* a = (GAS u32*)(ws + WS_RK + (p * 4 + l) * 4); const u32 d = smw[4 + l]; asm volatile("global_atomic_swap %0, %1, off sc1" :: "v"(a), "v"(d) : "memory"); }
        u32 a = 0, b = 0, c = 0;
#pragma unroll
        for (int j = 0; j < 8; ++j) { const u32 k = ((j & 1) ? (kv[j >> 1] & 0xffff0000u) : (kv[j >> 1] << 16)) | (u32)((2047 - p * 8 - j) << 5);
            c = med3(b, c, k); b = med3(a, b, k); a = max(a, k); }
        const u32 dl = min((b >> 16) - (c >> 16), 511u);
        const u64 mine = ((u64)(b | (dl & 31u)) << 32) | (a | 16u | (dl >> 5));
        asm volatile("s_mov_b32 exec_hi, 0\n\ts_waitcnt vmcnt(0) lgkmcnt(0)\n\ts_barrier\n\tglobal_atomic_swap_x2 %0, %1, off sc1\n\ts_mov_b64 exec, -1" :: "v"(kslot), "v"(mine) : "memory");
    };
    if (colwg) {
        phase2(ws, sm, w8, l, grp, P0, [&]() {}, [&]() {
            init_state();
            if (w8 == 4) {
                const SyncOps so = sync_ops<2>(ws + WS_F1, p, rep, tag, l);
                bar(); drain();
                sync_arrive(so); sync_wait2<0>(so);
            } else if (w8 == 5) {
                const u32 fo = ((grp + 8 * (l >> 2)) * DF_ + 32 * rk + 1 + (l & 3) * 8) * 2;
                bar();
                __builtin_amdgcn_s_sleep(GDELAY);
#pragma unroll 1
                for (int i = 0; i < WB; ++i) { dma16(rFr, fo, i * 128 * DF_ * 2, L_WF + i * 1056); dma16(rFi, fo, i * 128 * DF_ * 2, L_WF + (16 + i) * 1056); }
                STAMP(1);
                bar();
                __builtin_amdgcn_s_sleep(1);
#pragma unroll 1
                for (int i = WB; i < 16; ++i) { dma16(rFr, fo, i * 128 * DF_ * 2, L_WF + i * 1056); dma16(rFi, fo, i * 128 * DF_ * 2, L_WF + (16 + i) * 1056); }
                drain();
                STAMP(2);
            } else { bar(); drain(); bar(); }
            asm volatile("" :: "v"(cpf));
            bar();
        } STARG);
        if (w8 == 4) {
            const SyncOps so = sync_ops<1>(ws + WS_F2, grp * 16 + rk, rep, tag, l);
            drain();
            sync_arrive(so); sync_wait1<0>(so);
            bar();
            if (blockIdx.x == 0 && l == 0) *(u32*)(ws + WS_EPOCH) = tag;
            bar(); bar();
            publish_keys();
            select8<6>(ws, 28 + (rk & 3), l, smw);
        } else {
            drain();
            Fill f = fill_init(wd + (size_t)grp * 448 * D_ + rk * 64, D_ * 2, 448, l);
            bar(); bar(); bar();
            if (w8 == 5) fill<60>(f, DB8);
            bar(); bar();
            if (w8 == 5) { __builtin_amdgcn_s_sleep(1); fill<DEPTHD>(f, 14 - DB8); drain(); STAMP(3); }
        }
        bar();
        down_phase(ws, out, sm, w8, l, grp, rk, [&]() {} STARG);
    } else if (w8 == 4) {
        init_state();
        { const SyncOps so = sync_ops<2>(ws + WS_F1, p, rep, tag, l);
          bar(); drain();
          asm volatile("" :: "v"(cpf));
          sync_arrive(so); }
        { const SyncOps so = sync_ops<1>(ws + WS_F2, 0, rep, tag, l);
          for (u32 sp = 0; sp < SPIN_CAP; ++sp) { if (*(volatile LAS u32*)(smw + 2) == tag) break; __builtin_amdgcn_s_sleep(4); }
          sync_wait1<0>(so); }
        bar();
        bar(); bar();
        publish_keys();
        STAMP(5);
        if (rk < 14) select8<0>(ws, (grp * 14 + rk) % 28, l, smw); else select8<6>(ws, 28 + (rk & 3), l, smw);
        STAMP(7);
        bar();
    } else if (w8 == 5) {
        { const u32 to = ((grp + 8 * (16 * rk + (l >> 3))) * DF_ + (l & 7) * 64) * 2, to2 = l < 16 ? ((grp + 8 * (16 * rk + l)) * DF_ + 512) * 2 : 0x7ffffff0u;
          __builtin_amdgcn_s_sleep(LDELAY);
          touch(rFr, to, 0, L_JUNK); touch(rFi, to, 0, L_JUNK); touch(rFr, to, 64 * DF_ * 2, L_JUNK); touch(rFi, to, 64 * DF_ * 2, L_JUNK);
          touch(rFr, to2, 0, L_JUNK); touch(rFi, to2, 0, L_JUNK); }
        init_state();
        const u16* wbase = ((rk & 1) ? wu : wg) + grp * 448;
        const int slow = (3 - (int)((u64)wbase >> 7)) & 7;
        Fill f = fill_init(wbase + (rk < 14 ? (rk >> 1) : min(slow, 6)) * 64, F_ * 2, 1024, l);
        const SyncOps so = sync_ops<2>(ws + WS_F1, 0, rep, tag, l);
        const int on = rk < 14 ? 1 : 0;
        auto assist = [&](int i0, int i1) {
            if (rk >= 14 && slow < 7)
#pragma unroll 1
                for (int i = i0; i < i1; ++i) { touch(f.r, l * (F_ * 2), i * 64 * F_ * 2, L_JUNK); asm volatile("s_waitcnt vmcnt(2)" ::: "memory"); }
        };
        if (l == 0) smw[2] = tag - 1u;
        STAMP(1);
        bar(); bar();
        __builtin_amdgcn_s_sleep(1);
        fill<DEPTH>(f, on * (NS1 / 4));
        assist(AS0, 16);
        STAMP(2);
        if (NS1 < 128 && rk < 14 && (rk >> 1) != slow) { sync_wait2<4>(so); __builtin_amdgcn_s_sleep(TS2); }
        STAMP(3);
        fill<DEPTH>(f, on * ((NS2 - NS1) / 4));
        {
          const v4u rD = mkrsrc(wd + (size_t)grp * 448 * D_, 448u * D_ * 2); const u32 to = (4 * rk + (l >> 4)) * (D_ * 2) + (l & 15) * 128;
#pragma unroll 1
          for (int i = 0; i < ((rk >> 1) == slow ? 0 : NDT); ++i) { touch(rD, to, i * 64 * D_ * 2, L_JUNK); asm volatile("s_waitcnt vmcnt(3)" ::: "memory"); } }
        if (l == 0) *(volatile LAS u32*)(smw + 2) = tag;
        STAMP(4);
        bar(); bar(); bar(); bar();
        __builtin_amdgcn_s_sleep(1);
        fill<DEPTH>(f, on * ((128 - NS2) / 4));
        drain();
        asm volatile("" :: "v"(cpf));
        STAMP(8);
        if (l == 0) *(volatile LAS u32*)smw = 1u;
        asm volatile("s_waitcnt lgkmcnt(0)" ::: "memory");
    } else { drain(); asm volatile("" :: "v"(cpf)); return; }
    STAMP_OUT;
    }
}
}

extern "C" void kernel_launch(void* const* d_in, const int* in_sizes, int n_in, void* d_out, int out_size, void* d_ws, size_t ws_size, hipStream_t stream) {
    (void)in_sizes; (void)n_in; (void)out_size; (void)ws_size;
    (void)hipFuncSetAttribute((const void*)fused, hipFuncAttributeMaxDynamicSharedMemorySize, L_TOTAL);
    fused<<<256, 512, L_TOTAL, stream>>>((const u16*)d_in[0], (const u16*)d_in[1], (const u16*)d_in[2], (const u16*)d_in[4],
                                         (const u16*)d_in[5], (const u16*)d_in[6], (const u16*)d_in[7], (u16*)d_out, (char*)d_ws);
}
```

```cpp
#include <hip/hip_runtime.h>
#include <stdint.h>

typedef unsigned short u16;
typedef unsigned u32;
typedef unsigned long long u64;
typedef u32 v4u __attribute__((ext_vector_type(4)));
typedef u32 v2u __attribute__((ext_vector_type(2)));
typedef float f2 __attribute__((ext_vector_type(2)));
typedef float f4 __attribute__((ext_vector_type(4)));
typedef float f16v __attribute__((ext_vector_type(16)));
typedef _Float16 h2 __attribute__((ext_vector_type(2)));
typedef _Float16 h4 __attribute__((ext_vector_type(4)));
typedef _Float16 h8 __attribute__((ext_vector_type(8)));
typedef __bf16 b2 __attribute__((ext_vector_type(2)));
typedef __bf16 b8 __attribute__((ext_vector_type(8)));
typedef short s4 __attribute__((ext_vector_type(4)));
#define DI __device__ __forceinline__
#define GAS __attribute__((address_space(1)))
#define LAS __attribute__((address_space(3)))
#define BC(T, x) __builtin_bit_cast(T, x)

#define LDELAY 12
#define GDELAY 7
#define WB 8
#define NS1 32
#define TS2 16
#define NS2 128
#define DB8 0
#define AS0 7
#define NDT 0
#define DEPTH 28
#define DEPTHD 28
#define STR_(x) #x
#define STR(x) STR_(x)

namespace {
constexpr int S_ = 2048, D_ = 1024, DF_ = 513, F_ = 3584;
constexpr size_t WS_EPOCH = 0;
constexpr size_t WS_F1 = 4096;
constexpr size_t WS_F2 = WS_F1 + 32 * 16 * 256;
constexpr size_t WS_KEY = WS_F2 + 32 * 8 * 256;
constexpr size_t WS_FLAG2 = WS_KEY + 32 * 32 * 256;
constexpr size_t WS_RK = WS_FLAG2 + 8 * 4 * 256;
constexpr size_t WS_ST = WS_RK + 4096;
constexpr size_t WS_ACT = WS_ST + 1024 * 64;
constexpr size_t WS_ACC = WS_ACT + 8 * 2 * 7 * 1024;
constexpr int EC = 16384 + 128;
constexpr size_t WS_E = 10 << 20;
constexpr size_t WS_H = 5 << 20;
constexpr size_t WS_TS = WS_H + (4 << 20);
static_assert(WS_ACC + 65536 <= WS_H, "map");
constexpr int RU = 17408 + 128;
constexpr int L_W = 0;
constexpr int L_R1 = 0;
constexpr int L_WF = 2 * RU;
constexpr int L_R2 = L_WF + 32 * 1056;
constexpr int L_R0 = 131072;
constexpr int L_SM = L_R0 + 17408;
constexpr int L_TW = L_SM + 256;
constexpr int L_JUNK = L_TW + 8192;
constexpr int L_TOTAL = L_JUNK + 256;
static_assert(L_R2 + 2 * RU <= L_R0 && L_TOTAL <= 163840, "lds");
constexpr u32 SPIN_CAP = 1u << 16;
constexpr int CODE_BYTES = 39 * 1024;

DI float u2f(u32 x) { return BC(float, x); }
DI u32 f2u(float x) { return BC(u32, x); }
DI u32 pkh(float a, float b) { f2 v = {a, b}; return BC(u32, __builtin_convertvector(v, h2)); }
DI u32 pkb(float a, float b) { f2 v = {a, b}; return BC(u32, __builtin_convertvector(v, b2)); }
DI u32 rotp(u32 m) { const h2 v = BC(h2, m), k = {(_Float16)-1.f, (_Float16)1.f}; const h2 r = v.yx * k; return BC(u32, r); }
DI u32 rotm(u32 m) { const h2 v = BC(h2, m), k = {(_Float16)1.f, (_Float16)-1.f}; const h2 r = v.yx * k; return BC(u32, r); }
DI float rbf(float a) { return u2f(pkb(a, a) << 16); }
DI float blo(u32 d) { return u2f(d << 16); }
DI float bhi(u32 d) { return u2f(d & 0xffff0000u); }
DI float dot2(u32 a, u32 b, float c) { return __builtin_amdgcn_fdot2_f32_bf16(BC(b2, a), BC(b2, b), c, false); }
DI f16v mm32h(v4u a, v4u b, f16v c) { return __builtin_amdgcn_mfma_f32_32x32x16_f16(BC(h8, a), BC(h8, b), c, 0, 0, 0); }
DI f16v mm32b(v4u a, v4u b, f16v c) { return __builtin_amdgcn_mfma_f32_32x32x16_bf16(BC(b8, a), BC(b8, b), c, 0, 0, 0); }
DI f4 mm16h(v4u a, v4u b, f4 c) { return __builtin_amdgcn_mfma_f32_16x16x32_f16(BC(h8, a), BC(h8, b), c, 0, 0, 0); }
DI f4 mm16b(v4u a, v4u b, f4 c) { return __builtin_amdgcn_mfma_f32_16x16x32_bf16(BC(b8, a), BC(b8, b), c, 0, 0, 0); }
DI f4 mm16k16(v2u a, v2u b, f4 c) { return __builtin_amdgcn_mfma_f32_16x16x16f16(BC(h4, a), BC(h4, b), c, 0, 0, 0); }
DI v2u trd(LAS char* p) { return BC(v2u, __builtin_amdgcn_ds_read_tr16_b64_v4i16((LAS s4*)p)); }
DI void bar() { asm volatile("s_waitcnt lgkmcnt(0)\n\ts_barrier" ::: "memory"); }
DI void drain() { asm volatile("s_waitcnt vmcnt(0)" ::: "memory"); }
DI float sinr(float r) { return __builtin_amdgcn_sinf(r); }
DI float cosr(float r) { return __builtin_amdgcn_cosf(r); }
DI float rcpf(float x) { return __builtin_amdgcn_rcpf(x); }
DI float sigm(float x) { return rcpf(1.f + __builtin_amdgcn_exp2f(-1.44269504f * x)); }
template <int CTRL> DI float dppf(float v) { return u2f((u32)__builtin_amdgcn_update_dpp(0, (int)f2u(v), CTRL, 0xf, 0xf, true)); }
template <int CTRL> DI u32 dppu(u32 v) { return (u32)__builtin_amdgcn_update_dpp(0, (int)v, CTRL, 0xf, 0xf, true); }
DI float rowsum(float v) { v += dppf<0x128>(v); v += dppf<0x124>(v); v += dppf<0x122>(v); v += dppf<0x121>(v); return v; }
DI float swapadd32(float a, float b) { auto r = __builtin_amdgcn_permlane32_swap(f2u(a), f2u(b), false, false); const u32 x = r[0], y = r[1]; return u2f(x) + u2f(y); }
DI float swapadd16(float a, float b) { auto r = __builtin_amdgcn_permlane16_swap(f2u(a), f2u(b), false, false); const u32 x = r[0], y = r[1]; return u2f(x) + u2f(y); }
DI float rdl(float v, int lane) { return u2f((u32)__builtin_amdgcn_readlane((int)f2u(v), lane)); }
DI u32 rdlu(u32 v, int lane) { return (u32)__builtin_amdgcn_readlane((int)v, lane); }
DI float red4(float a, float b, float c, float d) { return rowsum(swapadd16(swapadd32(a, b), swapadd32(c, d))); }
DI u32 med3(u32 a, u32 b, u32 c) { return max(min(a, b), min(max(a, b), c)); }
DI int swap23(int v) { return (v & 0x13) | ((v & 4) << 1) | ((v & 8) >> 1); }
#define RSRC(p, n) __builtin_amdgcn_make_buffer_rsrc((void*)(p), 0, (n), 0x00020000)
#define PIN(x) asm volatile("" : "+v"(x))
#define SB() __builtin_amdgcn_sched_barrier(0)
constexpr int AUX_NT = 2, AUX_SC1 = 16;

#define STAMP(i) do {} while (0)
#define STAMP_DECL do {} while (0)
#define STAMP_OUT do {} while (0)

DI void ln_scalars(float red, float& mean, float& sd) {
    const float sq = u2f((u32)__builtin_amdgcn_permlane16_swap(f2u(red), f2u(red), false, false)[1]);
    mean = red * (1.f / 1024.f);
    const float var = fmaxf(sq * (1.f / 1024.f) - mean * mean, 0.f);
    sd = rbf(__builtin_amdgcn_sqrtf(rbf(rbf(var) + 1e-5f)));
}

DI v4u mkrsrc(const void* p, u32 n) { const u64 a = (u64)p; return v4u{(u32)a, (u32)(a >> 32) & 0xffffu, n, 0x00020000u}; }
DI void dma16(v4u r, u32 voff, u32 soff, u32 lds) { asm volatile("s_mov_b32 m0, %3\n\ts_nop 0\n\tbuffer_load_dwordx4 %0, %1, %2 offen nt lds" :: "v"(voff), "s"(r), "s"(soff), "s"(lds) : "memory"); }
DI void touch(v4u r, u32 voff, u32 soff, u32 lds) { asm volatile("s_mov_b32 m0, %3\n\ts_nop 0\n\tbuffer_load_ushort %0, %1, %2 offen nt lds" :: "v"(voff), "s"(r), "s"(soff), "s"(lds) : "memory"); }

struct SyncOps { GAS u32* a; GAS u64* p0; u64 want, zero; u32 tag; };
template <int NP> DI SyncOps sync_ops(char* base, int id, int rep, u32 tag, int l) {
    SyncOps o; o.a = (GAS u32*)(base + (l & 31) * (NP * 2048) + (id >> 4) * 256 + (id & 15) * 4); o.p0 = (GAS u64*)(base + rep * (NP * 2048) + (l >> 3) * 256 + (l & 7) * 8);
    o.want = ((u64)tag << 32) | tag; o.zero = 0; o.tag = tag;
    PIN(o.a); PIN(o.p0); PIN(o.want); PIN(o.zero); PIN(o.tag);
    return o;
}
DI void sync_arrive(const SyncOps& o) {
    asm volatile("s_mov_b32 exec_hi, 0\n\ts_waitcnt lgkmcnt(0)\n\ts_barrier\n\tglobal_atomic_swap %0, %1, off sc1\n\ts_mov_b64 exec, -1" :: "v"(o.a), "v"(o.tag) : "memory");
}
template <int NAP> DI void sync_wait1(const SyncOps& o) {
    u64 v0; u32 cap = SPIN_CAP;
    asm volatile(".Lp%=:\n\t"
                 "global_atomic_add_x2 %0, %2, %3, off sc0 sc1\n\t"
                 "s_sub_u32 %1, %1, 1\n\ts_waitcnt vmcnt(0)\n\ts_cbranch_scc1 .Ld%=\n\t"
                 "v_cmp_ne_u64 vcc, %0, %4\n\ts_and_b64 exec, exec, vcc\n\ts_cbranch_scc0 .Ld%=\n\ts_sleep %5\n\ts_branch .Lp%=\n"
                 ".Ld%=:\n\ts_mov_b64 exec, -1"
                 : "=&v"(v0), "+s"(cap) : "v"(o.p0), "v"(o.zero), "v"(o.want), "i"(NAP) : "memory", "vcc", "scc");
}
template <int NAP> DI void sync_wait2(const SyncOps& o) {
    u64 v0, v1, m0, m1; u32 cap = SPIN_CAP;
    asm volatile("s_mov_b64 %2, -1\n\ts_mov_b64 %3, -1\n"
                 ".Lp%=:\n\t"
                 "s_mov_b64 exec, %2\n\ts_cbranch_execz .La%=\n\t"
                 "global_atomic_add_x2 %0, %5, %6, off sc0 sc1\n"
                 ".La%=:\n\t"
                 "s_mov_b64 exec, %3\n\ts_cbranch_execz .Lb%=\n\t"
                 "global_atomic_add_x2 %1, %5, %6, off offset:2048 sc0 sc1\n"
                 ".Lb%=:\n\t"
                 "s_mov_b64 exec, -1\n\t"
                 "s_sub_u32 %4, %4, 1\n\t"
                 "s_waitcnt vmcnt(0)\n\t"
                 "s_cbranch_scc1 .Ld%=\n\t"
                 "v_cmp_ne_u64 %2, %0, %7\n\tv_cmp_ne_u64 %3, %1, %7\n\t"
                 "s_or_b64 vcc, %2, %3\n\t"
                 "s_cbranch_scc0 .Ld%=\n\ts_sleep %8\n\ts_branch .Lp%=\n"
                 ".Ld%=:"
                 : "=&v"(v0), "=&v"(v1), "=&s"(m0), "=&s"(m1), "+s"(cap) : "v"(o.p0), "v"(o.zero), "v"(o.want), "i"(NAP) : "memory", "vcc", "scc");
}
struct Fill { v4u r; u32 a, b, so, m0, step; };
DI Fill fill_init(const u16* base, u32 rowbytes, u32 rows, int l) {
    Fill f; f.r = mkrsrc(base, (rows - 1) * rowbytes + 128u); f.a = (l >> 3) * rowbytes + ((l & 7) ^ ((l >> 3) & 2)) * 16; f.b = f.a ^ 64u;
    f.step = 8u * rowbytes; f.so = 0u - f.step; f.m0 = L_W - 1024; return f;
}
template <int DP> DI void fill(Fill& f, int n) {
    if (n > 0)
        asm volatile("s_mov_b32 m0, %3\n"
                 ".Lf%=:\n\t"
                 "s_add_u32 m0, m0, 0x400\n\ts_add_u32 %2, %2, %6\n\tbuffer_load_dwordx4 %0, %5, %2 offen nt lds\n\t"
                 "s_add_u32 m0, m0, 0x400\n\ts_add_u32 %2, %2, %6\n\tbuffer_load_dwordx4 %1, %5, %2 offen nt lds\n\t"
                 "s_add_u32 m0, m0, 0x400\n\ts_add_u32 %2, %2, %6\n\tbuffer_load_dwordx4 %0, %5, %2 offen nt lds\n\t"
                 "s_add_u32 m0, m0, 0x400\n\ts_add_u32 %2, %2, %6\n\tbuffer_load_dwordx4 %1, %5, %2 offen nt lds\n\t"
                 "s_sub_u32 %4, %4, 1\n\tv_swap_b32 %0, %1\n\ts_waitcnt vmcnt(%7)\n\ts_cmp_lg_u32 %4, 0\n\ts_cbranch_scc1 .Lf%=\n\t"
                 "s_mov_b32 %3, m0"
                 : "+v"(f.a), "+v"(f.b), "+s"(f.so), "+s"(f.m0), "+s"(n) : "s"(f.r), "s"(f.step), "i"(DP) : "memory", "scc");
}

template <int NAP> DI void select8(char* ws, int rep, int l, LAS u32* smw) {
        u64 v0, v1, v2, v3;
        { GAS u64* p0 = (GAS u64*)(ws + WS_KEY + rep * 8192 + 4096 + (l >> 3) * 256 + (l & 7) * 8); const u64 zero = 0;
          u64 m0, m1, m2, m3; u32 cap = SPIN_CAP;
          asm volatile("s_mov_b64 %4, -1\n\ts_mov_b64 %5, -1\n\ts_mov_b64 %6, -1\n\ts_mov_b64 %7, -1\n"
                 ".Lp%=:\n\t"
                 "s_mov_b64 exec, %4\n\ts_cbranch_execz .La%=\n\t"
                 "global_atomic_add_x2 %0, %9, %10, off offset:-4096 sc0 sc1\n"
                 ".La%=:\n\t"
                 "s_mov_b64 exec, %5\n\ts_cbranch_execz .Lb%=\n\t"
                 "global_atomic_add_x2 %1, %9, %10, off offset:-2048 sc0 sc1\n"
                 ".Lb%=:\n\t"
                 "s_mov_b64 exec, %6\n\ts_cbranch_execz .Lc%=\n\t"
                 "global_atomic_add_x2 %2, %9, %10, off sc0 sc1\n"
                 ".Lc%=:\n\t"
                 "s_mov_b64 exec, %7\n\ts_cbranch_execz .Le%=\n\t"
                 "global_atomic_add_x2 %3, %9, %10, off offset:2048 sc0 sc1\n"
                 ".Le%=:\n\t"
                 "s_mov_b64 exec, -1\n\t"
                 "s_sub_u32 %8, %8, 1\n\t"
                 "s_waitcnt vmcnt(0)\n\t"
                 "s_cbranch_scc1 .Ld%=\n\t"
                 "v_cmp_eq_u64 %4, %0, %10\n\tv_cmp_eq_u64 %5, %1, %10\n\tv_cmp_eq_u64 %6, %2, %10\n\tv_cmp_eq_u64 %7, %3, %10\n\t"
                 "s_or_b64 vcc, %4, %5\n\ts_or_b64 vcc, vcc, %6\n\ts_or_b64 vcc, vcc, %7\n\t"
                 "s_cbranch_scc0 .Ld%=\n\ts_sleep %11\n\ts_branch .Lp%=\n"
                 ".Ld%=:"
                 : "=&v"(v0), "=&v"(v1), "=&v"(v2), "=&v"(v3), "=&s"(m0), "=&s"(m1), "=&s"(m2), "=&s"(m3), "+s"(cap) : "v"(p0), "v"(zero), "i"(NAP) : "memory", "vcc", "scc"); }
        {
          const u32 f0 = (u32)v0, f1 = (u32)v1, f2_ = (u32)v2, f3 = (u32)v3, s0 = (u32)(v0 >> 32), s1 = (u32)(v1 >> 32), s2 = (u32)(v2 >> 32), s3 = (u32)(v3 >> 32);
          u32 a = f0, b = s0, c = 0;
#define INS(k) do { c = med3(b, c, k); b = med3(a, b, k); a = max(a, k); } while (0)
          INS(f1); INS(f2_); INS(f3); INS(s1); INS(s2); INS(s3);
          const u32 a16 = a >> 16, b16 = b >> 16;
          u32 gm = max(a16, dppu<0xb1>(a16)); gm = max(gm, dppu<0x4e>(gm)); gm = max(gm, dppu<0x141>(gm));
          const u32 ot = dppu<0x128>(gm), mx = max(gm, ot), mn = min(gm, ot);
          u32 lo;
          { u32 hi, mid, c1, c2; u64 n1;
            asm volatile("s_nop 0\n\t"
                 "v_readlane_b32 %0, %8, 0\n\tv_readlane_b32 %2, %8, 16\n\tv_readlane_b32 %3, %8, 32\n\tv_readlane_b32 %4, %8, 48\n\t"
                 "s_min_u32 %0, %0, %2\n\ts_min_u32 %3, %3, %4\n\tv_readlane_b32 %1, %9, 0\n\tv_readlane_b32 %2, %9, 16\n\ts_min_u32 %0, %0, %3\n\t"
                 "v_readlane_b32 %3, %9, 32\n\tv_readlane_b32 %4, %9, 48\n\t"
                 "s_max_u32 %1, %1, %2\n\ts_max_u32 %3, %3, %4\n\ts_max_u32 %1, %1, %3\n\ts_add_u32 %1, %1, 1\n"
                 ".Lq%=:\n\t"
                 "s_add_u32 %2, %0, %1\n\ts_lshr_b32 %2, %2, 1\n\t"
                 "v_cmp_le_u32 vcc, %2, %6\n\tv_cmp_le_u32 %5, %2, %7\n\t"
                 "s_cmp_eq_u32 %2, %0\n\ts_cbranch_scc1 .Lz%=\n\t"
                 "s_bcnt1_i32_b64 %3, vcc\n\ts_bcnt1_i32_b64 %4, %5\n\ts_add_u32 %3, %3, %4\n\t"
                 "s_cmp_ge_u32 %3, 8\n\ts_cselect_b32 %0, %2, %0\n\ts_cselect_b32 %1, %1, %2\n\t"
                 "s_cmp_lg_u32 %3, 8\n\ts_cbranch_scc1 .Lq%=\n"
                 ".Lz%=:"
                 : "=&s"(lo), "=&s"(hi), "=&s"(mid), "=&s"(c1), "=&s"(c2), "=&s"(n1) : "v"(a16), "v"(b16), "v"(mn), "v"(mx) : "vcc", "scc"); }
          const u64 ga = __ballot(a16 > lo), gb = __ballot(b16 > lo), ea = __ballot(a16 == lo), eb = __ballot(b16 == lo);
          const u32 n0 = __popcll(ga), n1_ = n0 + __popcll(gb), n2 = n1_ + __popcll(ea);
#define MBC(m, base) __builtin_amdgcn_mbcnt_hi((u32)((m) >> 32), __builtin_amdgcn_mbcnt_lo((u32)(m), (base)))
          const u32 ia = a16 > lo ? MBC(ga, 0) : MBC(ea, n1_), ib = b16 > lo ? MBC(gb, n0) : MBC(eb, n2);
          bool amb = (c >> 16) > lo;
          if (__builtin_expect(gb != 0, 0)) {
#define THIRD(f, s) ((((s) >> 16) - ((((f) & 15u) << 5) | ((s) & 31u))) > lo)
              amb = amb || THIRD(f0, s0) || THIRD(f1, s1) || THIRD(f2_, s2) || THIRD(f3, s3); }
          if (__builtin_expect(!__any(amb), 1)) {
              if (a16 >= lo && ia < 8) smw[8 + ia] = a;
              if (b16 >= lo && ib < 8) smw[8 + ib] = b;
          } else {
              auto rk = RSRC(ws + WS_RK, 4096);
              v4u kv[4];
#pragma unroll
              for (int i = 0; i < 4; ++i) kv[i] = __builtin_amdgcn_raw_buffer_load_b128(rk, (i * 64 + l) * 16, 0, AUX_SC1);
              u32 prev = 0xffffffffu;
#pragma unroll 1
              for (int n = 0; n < 8; ++n) { u32 best = 0;
#pragma unroll
                  for (int j = 0; j < 32; ++j) { const u32 d = kv[j >> 3][(j >> 1) & 3], k = ((j & 1) ? (d & 0xffff0000u) : (d << 16)) | (u32)((2047 - ((j >> 3) * 64 + l) * 8 - (j & 7)) << 5);
                      best = max(best, k < prev ? k : 0u); }
                  best = max(best, dppu<0x128>(best)); best = max(best, dppu<0x124>(best)); best = max(best, dppu<0x122>(best)); best = max(best, dppu<0x121>(best));
                  prev = max(max(rdlu(best, 0), rdlu(best, 16)), max(rdlu(best, 32), rdlu(best, 48)));
                  if (l == 0) smw[8 + n] = prev; }
          } }
}

template <typename F0, typename F> DI void phase2(char* ws, LAS char* sm, int w8, int l, int grp, int P0, F0 mid0, F mid
    ) {
        const int l15 = l & 15, g4 = l >> 4; const f4 zero4 = {0.f, 0.f, 0.f, 0.f};
        auto rE = RSRC(ws + WS_E, 256 * EC);
        v4u A1[2], A4[2], A2[2][2][2], A3[2][2];
        { float jb = (float)(16 * w8 + grp + 128 * l15) * (1.f / 2048.f); PIN(jb);
#pragma unroll
          for (int i = 0; i < 4; ++i) { const float bf = (float)(4 * g4 + i), a0 = bf * jb;
#pragma unroll
            for (int tau = 0; tau < 2; ++tau) { const float a = bf * (float)(tau * (8.f / 2048.f)) + a0, c = cosr(a), s = sinr(a); const u32 m = pkh(c, s), n = pkh(-s, c);
                A2[tau][0][0][i] = m; A2[tau][0][1][i] = n; A2[tau][1][0][i ^ 1] = m; A2[tau][1][1][i ^ 1] = n; } }
          const float c8 = (float)(8 * l15 + grp) * (1.f / 128.f);
#pragma unroll
          for (int i = 0; i < 4; ++i) { const float a = (float)(4 * g4 + i) * c8, c = cosr(a) * 0.125f, s = sinr(a) * 0.125f; A1[0][i] = pkh(c, s); A1[1][i] = pkh(-s, c); }
          PIN(A1[0]); PIN(A1[1]);
#pragma unroll
          for (int tau = 0; tau < 2; ++tau) { PIN(A2[tau][0][0]); PIN(A2[tau][0][1]); PIN(A2[tau][1][0]); PIN(A2[tau][1][1]); }
          SB();
          mid0();
          float bf = (float)l15 * (1.f / 2048.f); PIN(bf); const float a0 = bf * (float)(16 * w8 + grp + 512 * g4);
#pragma unroll
          for (int i = 0; i < 4; ++i)
#pragma unroll
            for (int tau = 0; tau < 2; ++tau) { const float a = bf * (float)(8 * tau + 128 * i) + a0, c = cosr(a), s = sinr(a); A3[tau][0][i] = pkh(c, -s); A3[tau][1][i] = pkh(s, c); }
          const float b8 = bf * 16.f, a8 = b8 * (float)(32 * g4 + grp);
#pragma unroll
          for (int i = 0; i < 4; ++i) { const float a = b8 * (float)(8 * i) + a8, c = cosr(a) * 0.125f, s = sinr(a) * 0.125f; A4[0][i] = pkh(c, -s); A4[1][i] = pkh(s, c); } }
        int goff = (64 * g4 + 2 * w8 + ((l >> 3) & 1)) * EC + P0 * 64 + (l & 7) * 16;
        int a1w0 = L_R1 + (g4 & 1) * RU + l15 * 1040 + ((2 * (w8 & 1) + (g4 >> 1)) ^ (g4 & 1)) * 4 + (w8 >> 1) * 16, a1w1 = a1w0 + (((w8 >> 1) ^ 2) - (w8 >> 1)) * 16;
        int a1r = L_R1 + 2 * w8 * 1040 + l15 * 64 + (g4 ^ ((l15 >> 3) << 1)) * 16;
        int a2w = L_R2 + 4 * g4 * 1088 + l15 * 64 + ((w8 >> 1) ^ ((l15 >> 1) & 3) ^ (l15 >> 3)) * 16 + (w8 & 1) * 8;
        const int tq = l & 3, sl = g4 ^ ((2 * tq) & 3) ^ (tq >> 1);
        int a2r0 = L_R2 + ((l >> 2) & 1) * RU + (2 * w8 + ((l >> 3) & 1)) * 1088 + tq * 256 + sl * 16, a2r1 = a2r0 ^ 16;
        int fb = L_WF + (4 * g4 + (l15 >> 2)) * 1056 + 2 * w8 * 64 + (l15 & 3) * 8;
        PIN(A1[0]); PIN(A1[1]); PIN(A4[0]); PIN(A4[1]);
#pragma unroll
        for (int tau = 0; tau < 2; ++tau) { PIN(A2[tau][0][0]); PIN(A2[tau][0][1]); PIN(A2[tau][1][0]); PIN(A2[tau][1][1]); PIN(A3[tau][0]); PIN(A3[tau][1]); }
        PIN(goff); PIN(a1w0); PIN(a1w1); PIN(a1r); PIN(a2w); PIN(a2r0); PIN(a2r1); PIN(fb);
        SB();
        mid();
        STAMP(6);
        v4u v[4];
#pragma unroll
        for (int i = 0; i < 4; ++i) v[i] = __builtin_amdgcn_raw_buffer_load_b128(rE, goff, i * 16 * EC, AUX_SC1);
        SB();
        f4 fr[4], fi[4];
#pragma unroll
        for (int k = 0; k < 4; ++k) { const v2u a = trd(sm + fb + k * 32), c = trd(sm + fb + k * 32 + 16 * 1056);
            fr[k] = f4{blo(a[0]), bhi(a[0]), blo(a[1]), bhi(a[1])}; fi[k] = f4{blo(c[0]), bhi(c[0]), blo(c[1]), bhi(c[1])}; }
#pragma unroll
        for (int k = 0; k < 4; ++k) { PIN(fr[k]); PIN(fi[k]); }
        SB();
        f4 ar[4], ai[4];
#pragma unroll
        for (int e = 0; e < 4; ++e) { const v4u f = {v[0][e], v[1][e], v[2][e], v[3][e]}; ar[e] = mm16h(f, A1[0], zero4); ai[e] = mm16h(f, A1[1], zero4); }
        SB();
#pragma unroll
        for (int e = 0; e < 4; e += 2)
#pragma unroll
            for (int i = 0; i < 4; ++i) { LAS u32* d = (LAS u32*)(sm + (i < 2 ? a1w0 : a1w1) + (4 * i + e) * 64); d[0] = pkh(ar[e][i], ai[e][i]); d[16] = pkh(ar[e + 1][i], ai[e + 1][i]); }
        STAMP(7);
        bar();
#pragma unroll
        for (int k = 0; k < 4; ++k) v[k] = *(LAS v4u*)(sm + a1r + (k >> 1) * 1040 + (k & 1) * RU);
        SB();
#pragma unroll
        for (int k = 0; k < 4; ++k) { ar[k] = mm16h(A2[k >> 1][k & 1][0], v[k], zero4); ai[k] = mm16h(A2[k >> 1][k & 1][1], v[k], zero4); }
        SB();
        f4 yr[4], yi[4];
#pragma unroll
        for (int k = 0; k < 4; ++k) { yr[k] = ar[k] * fr[k] - ai[k] * fi[k]; yi[k] = ar[k] * fi[k] + ai[k] * fr[k]; }
        SB();
#pragma unroll
        for (int k = 0; k < 4; ++k) { const v4u y = {pkh(yr[k][0], yi[k][0]), pkh(yr[k][1], yi[k][1]), pkh(yr[k][2], yi[k][2]), pkh(yr[k][3], yi[k][3])};
            ar[k] = mm16h(A3[k >> 1][0], y, zero4); ai[k] = mm16h(A3[k >> 1][1], y, zero4); }
        SB();
#pragma unroll
        for (int u = 0; u < 2; ++u)
#pragma unroll
            for (int i = 0; i < 4; ++i) *(LAS v2u*)(sm + a2w + u * RU + i * 1088) = v2u{pkh(ar[u][i], ai[u][i]), pkh(ar[2 + u][i], ai[2 + u][i])};
        bar();
        STAMP(8);
#pragma unroll
        for (int e = 0; e < 4; ++e) v[e] = *(LAS v4u*)(sm + (e < 2 ? a2r0 : a2r1) + e * 64);
        SB();
#pragma unroll
        for (int e = 0; e < 4; ++e) { ar[e] = mm16h(A4[0], v[e], zero4); ai[e] = mm16h(A4[1], v[e], zero4); }
        SB();
#pragma unroll
        for (int i = 0; i < 4; ++i) v[i] = v4u{pkh(ar[0][i], ai[0][i]), pkh(ar[1][i], ai[1][i]), pkh(ar[2][i], ai[2][i]), pkh(ar[3][i], ai[3][i])};
#pragma unroll
        for (int i = 0; i < 4; ++i) __builtin_amdgcn_raw_buffer_store_b128(v[i], rE, goff, i * 16 * EC, AUX_SC1);
        SB();
        STAMP(9);
}
#define STARG

template <typename F> DI void down_phase(char* ws, u16* out, LAS char* sm, int w8, int l, int grp, int rk, F mid
    ) {
        const int l15 = l & 15, g4 = l >> 4, t = w8 * 64 + l; const f4 zero4 = {0.f, 0.f, 0.f, 0.f};
        LAS u32* smw = (LAS u32*)(sm + L_SM); const u32 tag = smw[1];
        auto rH = RSRC(ws + WS_H, 4 << 20); auto rS = RSRC(ws + WS_ST, 65536); auto rA = RSRC(ws + WS_ACT, 8 * 2 * 7 * 1024);
        const u32 ktok = smw[8 + w8]; const int ptok = 2047 - (int)((ktok >> 5) & 2047u);
        const int dcol = rk * 64 + l;
        const u32 hv = __builtin_amdgcn_raw_buffer_load_b16(rH, ptok * 2048 + ((dcol >> 3) & 3) * 512 + swap23(dcol >> 5) * 16 + (dcol & 7) * 2, 0, AUX_SC1);
        const u32 st = __builtin_amdgcn_raw_buffer_load_b32(rS, (ptok >> 1) * 64 + (ptok & 1) * 4, 0, AUX_SC1);
        SB();
        mid();
        SB();
        v4u Bf[2][4];
        { const int sw = ((l15 >> 3) | (((w8 ^ g4) & 1) << 1)) * 32; LAS char* b = sm + L_W + (32 * w8 + 8 * g4 + (l15 >> 2)) * 128 + (l15 & 3) * 8;
#pragma unroll
          for (int ct = 0; ct < 4; ++ct) { LAS char* b2 = b + ((ct * 32) ^ sw);
#pragma unroll
            for (int k = 0; k < 2; ++k) { LAS char* b3 = b2 + ((k == 1 && w8 < 6) ? 32768 : 0); const v2u lo = trd(b3), hi = trd(b3 + 512); Bf[k][ct] = v4u{lo[0], lo[1], hi[0], hi[1]}; } } }
        const u32 kb = ktok & 0xffff0000u; const float rsel = u2f((kb & 0x80000000u) ? (kb & 0x7fff0000u) : (~kb & 0xffff0000u));
        float sg = rbf(sigm(rsel));
        float hf = blo(hv), ln = rbf(rbf(hf - blo(st)) * rcpf(bhi(st)));
        int aoff = (grp * 2 + (l15 >> 3)) * 7168 + w8 * 512 + g4 * 128 + (l & 7) * 16, aoff1 = w8 < 6 ? aoff + 4096 : 0x7ffffff0;
        GAS u64* ap = (GAS u64*)(ws + WS_ACC) + rk * 512 + t;
        GAS u16* op = (GAS u16*)out + ((ptok >> 3) + 256 * (((ptok >> 1) & 3) + 4 * (ptok & 1))) * D_ + dcol;
        int pwr = L_R2 + w8 * 2176 + l15 * 272 + g4 * 16, prd = L_R2 + w8 * 272 + l * 4;
#pragma unroll
        for (int k = 0; k < 2; ++k) { PIN(Bf[k][0]); PIN(Bf[k][1]); PIN(Bf[k][2]); PIN(Bf[k][3]); }
        PIN(sg); PIN(ln); PIN(hf); PIN(aoff); PIN(aoff1); PIN(ap); PIN(op); PIN(pwr); PIN(prd);
        STAMP(17);
        if (w8 == 4) {
            GAS u32* pp = (GAS u32*)(ws + WS_FLAG2 + (grp * 4 + (rk & 3)) * 256 + l * 4);
            u32 v, cap = SPIN_CAP;
            asm volatile("s_lshr_b64 exec, -1, 8\n"
                 ".Lp%=:\n\t"
                 "global_atomic_add %0, %2, %3, off sc0 sc1\n\t"
                 "s_sub_u32 %1, %1, 1\n\t"
                 "s_waitcnt vmcnt(0)\n\t"
                 "s_cbranch_scc1 .Ld%=\n\t"
                 "v_cmp_ne_u32 vcc, %0, %4\n\t"
                 "s_and_b64 exec, exec, vcc\n\t"
                 "s_cbranch_scc1 .Lp%=\n"
                 ".Ld%=:\n\t"
                 "s_mov_b64 exec, -1"
                 : "=&v"(v), "+s"(cap) : "v"(pp), "v"(0u), "v"(tag) : "memory", "vcc", "scc");
        }
        bar();
        STAMP(18);
        v4u a[2];
        a[0] = __builtin_amdgcn_raw_buffer_load_b128(rA, aoff, 0, AUX_SC1);
        a[1] = __builtin_amdgcn_raw_buffer_load_b128(rA, aoff1, 0, AUX_SC1);
        f4 acc[4] = {zero4, zero4, zero4, zero4};
#pragma unroll
        for (int k = 0; k < 2; ++k) { v4u f;
#pragma unroll
            for (int d = 0; d < 4; ++d) { const float lo = blo(a[k][d]), hi = bhi(a[k][d]); f[d] = pkb(lo * dppf<0x128>(lo), hi * dppf<0x128>(hi)); }
#pragma unroll
            for (int ct = 0; ct < 4; ++ct) acc[ct] = mm16b(Bf[k][ct], f, acc[ct]); }
        STAMP(19);
        if (l15 < 8) {
#pragma unroll
            for (int ct = 0; ct < 4; ++ct) *(LAS f4*)(sm + pwr + ct * 64) = acc[ct];
        }
        bar();
        float ps[8];
#pragma unroll
        for (int ww = 0; ww < 8; ++ww) ps[ww] = *(LAS float*)(sm + prd + ww * 2176);
        const float sum = ((ps[0] + ps[1]) + (ps[2] + ps[3])) + ((ps[4] + ps[5]) + (ps[6] + ps[7]));
        const float lim = 4.0e9f; const double magic = 103079215104.0;
        const u64 i0 = BC(u64, (double)__builtin_amdgcn_fmed3f(sum, -lim, lim) + magic) + ((1ull << 60) + (1ull << 48) - BC(u64, magic));
        const u64 j0 = i0 + (BC(u64, magic) - (8ull << 60) - (8ull << 48));
        u64 o0;
        STAMP(20);
        asm volatile("global_atomic_add_x2 %0, %1, %2, off sc0 sc1\n\ts_waitcnt vmcnt(0)" : "=&v"(o0) : "v"(ap), "v"(i0) : "memory");
        const float y = rbf(rbf((float)(BC(double, o0 + j0) - magic)) * sg);
        if ((u32)(o0 >> 60) == 7) op[0] = (u16)pkb(hf + rbf(ln + y), 0.f);
}

__global__ void __launch_bounds__(512) fused(const u16* __restrict__ x, const u16* __restrict__ fwr, const u16* __restrict__ fwi, const u16* __restrict__ wrt,
                                             const u16* __restrict__ wg, const u16* __restrict__ wu, const u16* __restrict__ wd, u16* __restrict__ out, char* __restrict__ ws) {
    extern __shared__ __attribute__((aligned(16))) char smem[];
    const int t = threadIdx.x, w8 = __builtin_amdgcn_readfirstlane(t >> 6), l = t & 63;
    const int grp = blockIdx.x & 7, rk = blockIdx.x >> 4;
    const bool colwg = (blockIdx.x & 8) == 0;
    const int p = grp * 32 + (blockIdx.x >> 3), P0 = grp * 32 + 2 * rk;
    LAS char* sm = (LAS char*)smem;
    LAS u32* smw = (LAS u32*)(sm + L_SM);
    STAMP_DECL;
    STAMP(0);
    u32 cpf;
    { const int o = min((int)((blockIdx.x >> 3) + (t & 15) * 32) * 128, CODE_BYTES - 128);
      asm volatile("s_getpc_b64 vcc\n\ts_mov_b64 exec, 0x3ff\n\tglobal_load_dword %0, %1, vcc\n\ts_mov_b64 exec, -1" : "=&v"(cpf) : "v"(o) : "vcc"); }
    asm volatile("" :: "s"(x));

    if (__builtin_expect(w8 < 4, 1)) {
    const int w = w8, l15 = l & 15, g4 = l >> 4;
    const f4 zero4 = {0.f, 0.f, 0.f, 0.f};
    auto rE = RSRC(ws + WS_E, 256 * EC);
    __builtin_amdgcn_s_setprio(2);
    const int hh = l >> 5, pl = swap23(l & 31);
    auto rx = RSRC(x, S_ * D_ * 2);
    v4u xr[2][2];
    { const int vo = (p + 256 * w) * 2048 + 64 * pl + 16 * hh;
      xr[0][0] = __builtin_amdgcn_raw_buffer_load_b128(rx, vo, 0, AUX_NT);
      xr[1][0] = __builtin_amdgcn_raw_buffer_load_b128(rx, vo, 1024 * 2048, AUX_NT);
      xr[0][1] = __builtin_amdgcn_raw_buffer_load_b128(rx, vo + 32, 0, AUX_NT);
      xr[1][1] = __builtin_amdgcn_raw_buffer_load_b128(rx, vo + 32, 1024 * 2048, AUX_NT); }
    SB();
    v4u wq[2];
    { auto rw = RSRC(wrt, 2048); const int o = 64 * pl + 16 * hh; wq[0] = __builtin_amdgcn_raw_buffer_load_b128(rw, o, 0, 0); wq[1] = __builtin_amdgcn_raw_buffer_load_b128(rw, o + 32, 0, 0); }
    __builtin_amdgcn_sched_barrier(0);
    STAMP(21);

    v4u M1[4], M2[4]; f16v twc, tws;
    { const float plf = (float)pl * (1.f / 1024.f), b0 = plf * (float)(8 * hh), pl32 = plf * 32.f, b32 = b0 * 32.f;
      const u32 sgn = (pl & 1) ? 0x80008000u : 0u;
#pragma unroll
      for (int r = 0; r < 8; ++r) {
        const float a32 = pl32 * (float)r + b32, c = cosr(a32), s = sinr(a32);
        const u32 m1 = pkh(c, s), m2 = pkh(-s, c);
        M1[r >> 2][r & 3] = m1; M2[r >> 2][r & 3] = m2; M1[2 + (r >> 2)][r & 3] = m1 ^ sgn; M2[2 + (r >> 2)][r & 3] = m2 ^ sgn;
      }
#pragma unroll
      for (int r = 0; r < 16; ++r) { const float a1k = plf * (float)(16 * (r >> 3) + (r & 7)) + b0; twc[r] = cosr(a1k); tws[r] = sinr(a1k); }
      twc *= (1.f / 32.f); tws *= (1.f / 32.f); }
    v4u PI[2];
    { const int k = pl - 8 * hh; const u32 one = (k & 1) ? 0x3f800000u : 0x3f80u;
#pragma unroll
      for (int d = 0; d < 8; ++d) PI[d >> 2][d & 3] = (k >> 1) == 8 * (d >> 2) + (d & 3) ? one : 0u; }
    v2u D8f;
    {
      const float k = 0.35355339f;
      { const int q = l15 >> 1; const float e8 = (q & 1) ? -0.125f : 0.125f, th = (float)(q * g4) * 0.125f + ((l15 & 1) ? 0.25f : 0.f), es = (q & 1) ? -k : k;
        D8f[0] = pkh(k * cosr(th + e8), es * cosr(th - e8)); D8f[1] = pkh(k * cosr(th - e8), es * cosr(th + e8)); }
    }
#pragma unroll
    for (int i = 0; i < 4; ++i) { PIN(M1[i]); PIN(M2[i]); }
    PIN(twc); PIN(tws); PIN(PI[0]); PIN(PI[1]); PIN(D8f);
    if (w == 0) {
#pragma unroll
        for (int i = 0; i < 4; ++i) { *(LAS f4*)(sm + L_TW + i * 2048 + l * 16) = f4{twc[4 * i], twc[4 * i + 1], twc[4 * i + 2], twc[4 * i + 3]};
                                      *(LAS f4*)(sm + L_TW + i * 2048 + 1024 + l * 16) = f4{tws[4 * i], tws[4 * i + 1], tws[4 * i + 2], tws[4 * i + 3]}; }
    }
    f16v zero16;
#pragma unroll
    for (int i = 0; i < 16; ++i) zero16[i] = 0.f;
    int eoff = p * EC + g4 * 4096 + (128 * w + 4 * l15) * 4;
    int zlo = L_R0 + w * 4112 + (pl + 256 * hh - 1) * 8, zhi = L_R0 + w * 4112 + (511 - pl - 256 * hh) * 8 + 4 - 1792, zh0 = L_R0 + w * 4112 + (((pl + 256 * hh - 1) * 8) & 4095);
    int zrd = L_R0 + g4 * 4112 + (128 * w + 4 * l15) * 8;
    PIN(eoff); PIN(zlo); PIN(zhi); PIN(zh0); PIN(zrd);
    __builtin_amdgcn_sched_barrier(0);
    STAMP(1);

    {
        float s1a = 0.f, s2a = 0.f, s1b = 0.f, s2b = 0.f;
#pragma unroll
        for (int i = 0; i < 8; ++i) { const u32 a = xr[0][i >> 2][i & 3], b = xr[1][i >> 2][i & 3];
            s1a = dot2(a, 0x3f803f80u, s1a); s2a = dot2(a, a, s2a); s1b = dot2(b, 0x3f803f80u, s1b); s2b = dot2(b, b, s2b); }
        asm volatile("" :: "v"(cpf), "v"(s1a), "v"(s2b));
        STAMP(2);
        f16v xa = mm32b(xr[0][0], PI[0], zero16); f16v xb = mm32b(xr[1][0], PI[0], zero16);
        xa = mm32b(xr[0][1], PI[1], xa); xb = mm32b(xr[1][1], PI[1], xb);
        float mean, sd; ln_scalars(red4(s1a, s1b, s2a, s2b), mean, sd);
        const float rs = rcpf(sd), of = -mean * rs;
        const float ra = rdl(rs, 0), rb = rdl(rs, 32), oa = rdl(of, 0), ob = rdl(of, 32);
        xa = xa * ra + oa; xb = xb * rb + ob;
        v4u z[4];
#pragma unroll
        for (int r = 0; r < 16; ++r) z[r >> 2][r & 3] = pkh(xa[r], xb[r]);
        f16v tr = zero16, ti = zero16;
#pragma unroll
        for (int s = 0; s < 4; ++s) { tr = mm32h(z[s], M1[s], tr); ti = mm32h(z[s], M2[s], ti); }
        const f16v ur = tr * twc + ti * tws, ui = ti * twc - tr * tws;
#pragma unroll
        for (int r = 0; r < 16; ++r) z[r >> 2][r & 3] = pkh(ur[r], ui[r]);
        tr = zero16; ti = zero16;
#pragma unroll
        for (int s = 0; s < 4; ++s) { tr = mm32h(M1[s], z[s], tr); ti = mm32h(M2[s], z[s], ti); }
        u32 zz[16];
#pragma unroll
        for (int r = 0; r < 16; ++r) zz[r] = pkh(tr[r], ti[r]);
        if (l == 0) zz[0] = zz[8];
        *(LAS u32*)(sm + zh0) = zz[0];
#pragma unroll
        for (int r = 1; r < 8; ++r) *(LAS u32*)(sm + zlo + 256 * r) = zz[r];
#pragma unroll
        for (int r = 0; r < 8; ++r) *(LAS u32*)(sm + zhi + 1792 - 256 * r) = zz[8 + r];
        STAMP(23);
        bar();
        STAMP(3);
        v4u q[4];
#pragma unroll
        for (int i = 0; i < 4; ++i) q[i] = *(LAS v4u*)(sm + zrd + (i >> 1) * 512 + (i & 1) * 16);
        SB();
        f4 d[8];
#pragma unroll
        for (int i = 0; i < 8; ++i) d[i] = mm16k16(D8f, v2u{q[i >> 1][2 * (i & 1)], q[i >> 1][2 * (i & 1) + 1]}, zero4);
#pragma unroll
        for (int u = 0; u < 2; ++u) {
            const v4u o0 = {pkh(d[4 * u][0], d[4 * u][1]), pkh(d[4 * u + 1][0], d[4 * u + 1][1]), pkh(d[4 * u + 2][0], d[4 * u + 2][1]), pkh(d[4 * u + 3][0], d[4 * u + 3][1])};
            const v4u o1 = {pkh(d[4 * u][2], d[4 * u][3]), pkh(d[4 * u + 1][2], d[4 * u + 1][3]), pkh(d[4 * u + 2][2], d[4 * u + 2][3]), pkh(d[4 * u + 3][2], d[4 * u + 3][3])};
            __builtin_amdgcn_raw_buffer_store_b128(o0, rE, eoff + u * 256, 0, AUX_SC1);
            __builtin_amdgcn_raw_buffer_store_b128(o1, rE, eoff + u * 256 + 2048, 0, AUX_SC1);
        }
        __builtin_amdgcn_sched_barrier(0);
        STAMP(4);
    }
    __builtin_amdgcn_s_setprio(0);
    if (colwg) phase2(ws, sm, w8, l, grp, P0, [&]() { drain(); STAMP(5); bar(); }, [&]() { bar(); } STARG);
    else { drain(); STAMP(5); bar(); }
    f16v ha, hb;
#pragma unroll
    for (int r = 0; r < 16; r += 2) { const u32 a = xr[0][r >> 3][(r >> 1) & 3], b = xr[1][r >> 3][(r >> 1) & 3]; ha[r] = blo(a); ha[r + 1] = bhi(a); hb[r] = blo(b); hb[r + 1] = bhi(b); }
    float wsum;
    { float s = 0.f;
#pragma unroll
      for (int i = 0; i < 8; ++i) s = dot2(wq[i >> 2][i & 3], 0x3f803f80u, s);
      wsum = red4(s, s, s, s); }
    v2u D8i;
#pragma unroll
    for (int x2 = 0; x2 < 2; ++x2) { const int q = 2 * g4 + x2, j = (l15 & 3) ^ 1; const float e8 = x2 ? -0.125f : 0.125f, kk = 0.35355339f * 0.25f;
        const float th = (float)(q * (l15 >> 2)) * 0.125f + ((j == 0 || j == 3) ? e8 : -e8), sc = (x2 && (j & 1)) ? -kk : kk;
        D8i[x2] = pkh(sc * cosr(th), sc * cosr(th + 0.25f)); }
#pragma unroll
    for (int i = 0; i < 4; ++i) { const f4 c = *(LAS f4*)(sm + L_TW + i * 2048 + l * 16), s = *(LAS f4*)(sm + L_TW + i * 2048 + 1024 + l * 16);
#pragma unroll
        for (int j = 0; j < 4; ++j) { twc[4 * i + j] = c[j]; tws[4 * i + j] = s[j]; } }
    PIN(D8i); PIN(twc); PIN(tws);
    float nyq = (w == 3 && l15 == 15) ? 0.5f : 0.f;
    PIN(nyq);
    int zw = L_R0 + g4 * 4096 + (128 * w + 4 * l15) * 4;
    int ylo = L_R0 + w * 4096 + (pl + 256 * hh - 1) * 4, yhi = L_R0 + w * 4096 + 2048 + (511 - pl - 256 * hh) * 4 - 896;
    int ho = (p * 8 + 2 * w) * 2048 + l * 16;
    PIN(ha); PIN(hb); PIN(wsum); PIN(zw); PIN(ylo); PIN(yhi); PIN(ho);
    SB();
    if (colwg) { drain(); STAMP(10); bar(); }
    bar();
    __builtin_amdgcn_s_setprio(2);
    STAMP(11);
    v4u hA[2], hB[2];
    {
        v4u v[2][2];
#pragma unroll
        for (int u = 0; u < 2; ++u) { v[u][0] = __builtin_amdgcn_raw_buffer_load_b128(rE, eoff + u * 256, 0, AUX_SC1); v[u][1] = __builtin_amdgcn_raw_buffer_load_b128(rE, eoff + u * 256 + 2048, 0, AUX_SC1); }
        f4 d[8];
#pragma unroll
        for (int i = 0; i < 8; ++i) d[i] = mm16k16(D8i, v2u{v[i >> 2][0][i & 3], v[i >> 2][1][i & 3]}, zero4);
        d[7][2] += nyq * (d[7][0] - d[7][2]); d[7][3] += nyq * (d[7][1] - d[7][3]);
#pragma unroll
        for (int u = 0; u < 2; ++u) {
            *(LAS v4u*)(sm + zw + u * 256) = v4u{pkh(d[4 * u][0], d[4 * u][1]), pkh(d[4 * u + 1][0], d[4 * u + 1][1]), pkh(d[4 * u + 2][0], d[4 * u + 2][1]), pkh(d[4 * u + 3][0], d[4 * u + 3][1])};
            *(LAS v4u*)(sm + zw + u * 256 + 2048) = v4u{pkh(d[4 * u][2], d[4 * u][3]), pkh(d[4 * u + 1][2], d[4 * u + 1][3]), pkh(d[4 * u + 2][2], d[4 * u + 2][3]), pkh(d[4 * u + 3][2], d[4 * u + 3][3])}; }
        STAMP(12);
        bar();
        v4u z[4];
        { v2u y0, y1, y2, y3, y4, y5, y6, y7;
          asm volatile("ds_read2_b32 %2, %8 offset0:128 offset1:160\n\tds_read2_b32 %3, %8 offset0:192 offset1:224\n\t"
                       "ds_read2_b32 %4, %9 offset0:224 offset1:192\n\tds_read2_b32 %5, %9 offset0:160 offset1:128\n\t"
                       "ds_read2_b32 %6, %9 offset0:96 offset1:64\n\tds_read2_b32 %7, %9 offset0:32 offset1:0\n\t"
                       "ds_read2_b32 %0, %8 offset0:0 offset1:32\n\tds_read2_b32 %1, %8 offset0:64 offset1:96\n\t"
                       "s_waitcnt lgkmcnt(0)"
                       : "=&v"(y0), "=&v"(y1), "=&v"(y2), "=&v"(y3), "=&v"(y4), "=&v"(y5), "=&v"(y6), "=&v"(y7) : "v"(ylo), "v"(yhi) : "memory");
          z[0] = v4u{l == 0 ? 0u : y0[0], y0[1], y1[0], y1[1]}; z[1] = v4u{y2[0], y2[1], y3[0], y3[1]}; z[2] = v4u{y4[0], y4[1], y5[0], y5[1]}; z[3] = v4u{y6[0], y6[1], y7[0], y7[1]}; }
        f16v tr = zero16, ti = zero16;
#pragma unroll
        for (int s = 0; s < 4; ++s) { const int s_ = (s + 1) & 3; tr = mm32h(z[s_], M1[s_], tr); ti = mm32h(z[s_], M2[s_], ti); }
        const f16v ur = tr * twc + ti * tws, ui = ti * twc - tr * tws;
#pragma unroll
        for (int r = 0; r < 16; ++r) z[r >> 2][r & 3] = pkh(ur[r], ui[r]);
#pragma unroll
        for (int s = 0; s < 4; ++s) { hb = mm32h(z[s], M1[s], hb); ha = mm32h(z[s], M2[s], ha); }
#pragma unroll
        for (int r = 0; r < 16; r += 2) { hA[r >> 3][(r >> 1) & 3] = pkb(ha[r], ha[r + 1]); hB[r >> 3][(r >> 1) & 3] = pkb(hb[r], hb[r + 1]); }
    }
    auto rH = RSRC(ws + WS_H, 4 << 20);
    __builtin_amdgcn_raw_buffer_store_b128(hA[0], rH, ho, 0, AUX_SC1); __builtin_amdgcn_raw_buffer_store_b128(hA[1], rH, ho + 1024, 0, AUX_SC1);
    __builtin_amdgcn_raw_buffer_store_b128(hB[0], rH, ho + 2048, 0, AUX_SC1); __builtin_amdgcn_raw_buffer_store_b128(hB[1], rH, ho + 3072, 0, AUX_SC1);
    __builtin_amdgcn_sched_barrier(0);
    STAMP(13);
    float mA, mB, rsA, rsB;
    auto rS = RSRC(ws + WS_ST, 65536);
    {
        float s1a = 0.f, s2a = 0.f, s3a = 0.f, s1b = 0.f, s2b = 0.f, s3b = 0.f;
#pragma unroll
        for (int i = 0; i < 8; ++i) { const u32 a = hA[i >> 2][i & 3], b = hB[i >> 2][i & 3], q = wq[i >> 2][i & 3];
            s1a = dot2(a, 0x3f803f80u, s1a); s2a = dot2(a, a, s2a); s3a = dot2(a, q, s3a); s1b = dot2(b, 0x3f803f80u, s1b); s2b = dot2(b, b, s2b); s3b = dot2(b, q, s3b); }
        const float s3h = swapadd32(s3a, s3b), red3 = rowsum(swapadd16(s3h, s3h));
        float mean, sd; ln_scalars(red4(s1a, s1b, s2a, s2b), mean, sd);
        const float m = rbf(mean), rs = rcpf(sd);
        const u32 rb = pkb((red3 - m * wsum) * rs, 0.f) & 0xffffu;
        const u32 key = (rb ^ ((rb & 0x8000u) ? 0xffffu : 0x8000u));
        if ((l & 31) == 0) {
            *(LAS u16*)(sm + L_SM + 16 + w * 4 + hh * 2) = (u16)key;
            __builtin_amdgcn_raw_buffer_store_b32((f2u(m) >> 16) | f2u(sd), rS, (p * 4 + w) * 64 + hh * 4, 0, AUX_SC1);
        }
        mA = rdl(m, 0); mB = rdl(m, 32); rsA = rdl(rs, 0); rsB = rdl(rs, 32);
    }
    STAMP(14);
    bar();
    drain();
    bar();
    __builtin_amdgcn_s_setprio(0);
    STAMP(15);
    PIN(mA); PIN(mB);
    v4u oA[2], oB[2];
#pragma unroll
    for (int s2 = 0; s2 < 2; ++s2)
#pragma unroll
        for (int d = 0; d < 4; ++d) {
            { const f2 h = {blo(hA[s2][d]), bhi(hA[s2][d])}, c0 = h - mA; const u32 c = pkb(c0[0], c0[1]); const f2 n0 = f2{blo(c), bhi(c)} * rsA; const u32 n = pkb(n0[0], n0[1]); const f2 o = h + f2{blo(n), bhi(n)}; oA[s2][d] = pkb(o[0], o[1]); }
            { const f2 h = {blo(hB[s2][d]), bhi(hB[s2][d])}, c0 = h - mB; const u32 c = pkb(c0[0], c0[1]); const f2 n0 = f2{blo(c), bhi(c)} * rsB; const u32 n = pkb(n0[0], n0[1]); const f2 o = h + f2{blo(n), bhi(n)}; oB[s2][d] = pkb(o[0], o[1]); } }
    {
        LAS char* ob = sm + L_R0 + w * 4352;
#pragma unroll
        for (int s2 = 0; s2 < 2; ++s2) { const int o = 64 * pl + (((2 * s2 + hh) ^ (pl >> 1)) & 3) * 16; *(LAS v4u*)(ob + o) = oA[s2]; *(LAS v4u*)(ob + 2048 + o) = oB[s2]; }
        asm volatile("s_waitcnt lgkmcnt(0)" ::: "memory");
#pragma unroll
        for (int s2 = 0; s2 < 2; ++s2) { const int a_ = 16 * s2 + (l >> 2), o = 64 * a_ + (((l & 3) ^ (a_ >> 1)) & 3) * 16; oA[s2] = *(LAS v4u*)(ob + o); oB[s2] = *(LAS v4u*)(ob + 2048 + o); }
    }
    auto store_out = [&]() {
        const int pos = 2047 - (int)((smw[8 + (l & 7)] >> 5) & 2047u);
        const bool selA = __any(pos == p * 8 + 2 * w), selB = __any(pos == p * 8 + 2 * w + 1);
        auto ro = RSRC(out, S_ * D_ * 2); const int oo = (p + 256 * w) * 2048 + l * 16;
        if (!selA) { __builtin_amdgcn_raw_buffer_store_b128(oA[0], ro, oo, 0, AUX_SC1); __builtin_amdgcn_raw_buffer_store_b128(oA[1], ro, oo + 1024, 0, AUX_SC1); }
        if (!selB) { __builtin_amdgcn_raw_buffer_store_b128(oB[0], ro, oo, 1024 * 2048, AUX_SC1); __builtin_amdgcn_raw_buffer_store_b128(oB[1], ro, oo + 1024, 1024 * 2048, AUX_SC1); } };
    if (colwg) {
        bar();
        STAMP(16);
        down_phase(ws, out, sm, w8, l, grp, rk, store_out STARG);
    } else if (rk < 14) {
        for (u32 sp = 0; sp < SPIN_CAP; ++sp) { if (*(volatile LAS u32*)smw) break; __builtin_amdgcn_s_sleep(1); }
        asm volatile("" ::: "memory");
        auto rA = RSRC(ws + WS_ACT, 8 * 2 * 7 * 1024);
        v4u Bf[8][4];
        { const int sw = ((l15 >> 3) | (((w ^ g4) & 1) << 1)) * 32; LAS char* b = sm + L_W + (32 * g4 + 8 * w + (l15 >> 2)) * 128 + (l15 & 3) * 8;
#pragma unroll
          for (int ct = 0; ct < 4; ++ct) { LAS char* b2 = b + ((ct * 32) ^ sw);
#pragma unroll
            for (int k = 0; k < 8; ++k) { const v2u lo = trd(b2 + swap23(4 * k) * 4096), hi = trd(b2 + swap23(4 * k) * 4096 + 512); Bf[k][ct] = v4u{lo[0], lo[1], hi[0], hi[1]}; } } }
        __builtin_amdgcn_sched_barrier(0);
#pragma unroll
        for (int k = 0; k < 8; ++k) { PIN(Bf[k][0]); PIN(Bf[k][1]); PIN(Bf[k][2]); PIN(Bf[k][3]); }
        const u32 tag = smw[1];
        const int tok = (t >> 2) & 7;
        int xw = L_R0 + w * 4352 + (l >> 3) * 544 + (l & 7) * 16, xrd = (l15 < 8 ? L_R0 + w * 4352 + l15 * 544 : L_TW + w * 512) + g4 * 16;
        if (l < 32) *(LAS v4u*)(sm + L_TW + w * 512 + l * 16) = v4u{0x3f803f80u, 0x3f803f80u, 0x3f803f80u, 0x3f803f80u};
        int pwr = L_W + w * 2448 + l15 * 272 + g4 * 16, pcs = L_W + 8 * 272 + ((t >> 5) * 8 + (t & 3) * 2) * 4, prd = pcs + (tok - 8) * 272;
        PIN(xw); PIN(xrd); PIN(pwr); PIN(pcs); PIN(prd);
        STAMP(16);
        bar();
        STAMP(17);
        const int pos = 2047 - (int)((smw[8 + (l >> 3)] >> 5) & 2047u), ptok = 2047 - (int)((smw[8 + tok] >> 5) & 2047u);
        v4u a[8];
        const int ao = pos * 2048 + w * 512 + (l & 7) * 16;
#pragma unroll
        for (int k = 0; k < 4; ++k) a[k] = __builtin_amdgcn_raw_buffer_load_b128(rH, ao + k * 128, 0, AUX_SC1);
        const u32 st = __builtin_amdgcn_raw_buffer_load_b32(rS, (ptok >> 1) * 64 + (ptok & 1) * 4, 0, AUX_SC1);
#pragma unroll
        for (int k = 0; k < 4; ++k) *(LAS v4u*)(sm + xw + k * 128) = a[k];
        asm volatile("s_waitcnt lgkmcnt(0)" ::: "memory");
#pragma unroll
        for (int k = 0; k < 8; ++k) a[k] = *(LAS v4u*)(sm + xrd + k * 64);
        SB();
        f4 acc[4] = {zero4, zero4, zero4, zero4};
#pragma unroll
        for (int k = 0; k < 8; ++k)
#pragma unroll
            for (int ct = 0; ct < 4; ++ct) acc[ct] = mm16b(Bf[k][ct], a[k], acc[ct]);
        STAMP(18);
        if (l15 < 9) {
#pragma unroll
            for (int ct = 0; ct < 4; ++ct) *(LAS f4*)(sm + pwr + ct * 64) = acc[ct];
        }
        bar();
        f2 sum = {0.f, 0.f}, cs = {0.f, 0.f};
#pragma unroll
        for (int ww = 0; ww < 4; ++ww) { sum += *(LAS f2*)(sm + prd + ww * 2448); cs += *(LAS f2*)(sm + pcs + ww * 2448); }
        const float m = blo(st), rs = rcpf(bhi(st));
        u32 o = pkb((sum[0] - m * cs[0]) * rs, (sum[1] - m * cs[1]) * rs);
        if (!(rk & 1)) { const float g0 = blo(o), g1 = bhi(o); o = pkb(g0 * sigm(g0), g1 * sigm(g1)); }
        __builtin_amdgcn_raw_buffer_store_b32(o, rA, ((grp * 2 + (rk & 1)) * 7 + (rk >> 1)) * 1024 + t * 4, 0, AUX_SC1);
        STAMP(19);
        drain();
        if (l < 4) { GAS u32* fa = (GAS u32*)(ws + WS_FLAG2 + (grp * 4 + l) * 256 + (rk * 4 + w) * 4); asm volatile("global_atomic_swap %0, %1, off sc1" :: "v"(fa), "v"(tag) : "memory"); }
        STAMP(20);
        store_out();
    } else {
        bar();
        store_out();
    }
    STAMP(22);
    STAMP_OUT;
    } else {
    const int rep = (blockIdx.x & 7) | ((blockIdx.x >> 4) & 3) << 3;
    u32 tag = *(const __attribute__((address_space(4))) u32*)(ws + WS_EPOCH) + 1u;
    GAS u64* kslot = (GAS u64*)(ws + WS_KEY + (l & 31) * 8192 + (p >> 3) * 256 + (p & 7) * 8);
    const v4u rFr = mkrsrc(fwr, S_ * DF_ * 2), rFi = mkrsrc(fwi, S_ * DF_ * 2);
    auto init_state = [&]() {
        asm volatile("" : "+s"(tag));
        if (w8 == 4) {
            __builtin_amdgcn_raw_buffer_store_b32(0u, RSRC(ws + WS_ACC, 65536), p * 256 + l * 4, 0, AUX_SC1);
            if (l < 32) { const u64 z = 0; asm volatile("global_atomic_swap_x2 %0, %1, off sc1" :: "v"(kslot), "v"(z) : "memory"); }
            if (l == 0) { smw[0] = 0u; smw[1] = tag; }
        } };
    auto publish_keys = [&]() {
        const v4u kv = *(LAS v4u*)(smw + 4);
        if (l < 4) { GAS u32* a = (GAS u32*)(ws + WS_RK + (p * 4 + l) * 4); const u32 d = smw[4 + l]; asm volatile("global_atomic_swap %0, %1, off sc1" :: "v"(a), "v"(d) : "memory"); }
        u32 a = 0, b = 0, c = 0;
#pragma unroll
        for (int j = 0; j < 8; ++j) { const u32 k = ((j & 1) ? (kv[j >> 1] & 0xffff0000u) : (kv[j >> 1] << 16)) | (u32)((2047 - p * 8 - j) << 5);
            c = med3(b, c, k); b = med3(a, b, k); a = max(a, k); }
        const u32 dl = min((b >> 16) - (c >> 16), 511u);
        const u64 mine = ((u64)(b | (dl & 31u)) << 32) | (a | 16u | (dl >> 5));
        asm volatile("s_mov_b32 exec_hi, 0\n\ts_waitcnt vmcnt(0) lgkmcnt(0)\n\ts_barrier\n\tglobal_atomic_swap_x2 %0, %1, off sc1\n\ts_mov_b64 exec, -1" :: "v"(kslot), "v"(mine) : "memory");
    };
    if (colwg) {
        phase2(ws, sm, w8, l, grp, P0, [&]() {}, [&]() {
            init_state();
            if (w8 == 4) {
                const SyncOps so = sync_ops<2>(ws + WS_F1, p, rep, tag, l);
                bar(); drain();
                sync_arrive(so); sync_wait2<0>(so);
            } else if (w8 == 5) {
                const u32 fo = ((grp + 8 * (l >> 2)) * DF_ + 32 * rk + 1 + (l & 3) * 8) * 2;
                bar();
                __builtin_amdgcn_s_sleep(GDELAY);
#pragma unroll 1
                for (int i = 0; i < WB; ++i) { dma16(rFr, fo, i * 128 * DF_ * 2, L_WF + i * 1056); dma16(rFi, fo, i * 128 * DF_ * 2, L_WF + (16 + i) * 1056); }
                STAMP(1);
                bar();
                __builtin_amdgcn_s_sleep(1);
#pragma unroll 1
                for (int i = WB; i < 16; ++i) { dma16(rFr, fo, i * 128 * DF_ * 2, L_WF + i * 1056); dma16(rFi, fo, i * 128 * DF_ * 2, L_WF + (16 + i) * 1056); }
                drain();
                STAMP(2);
            } else { bar(); drain(); bar(); }
            asm volatile("" :: "v"(cpf));
            bar();
        } STARG);
        if (w8 == 4) {
            const SyncOps so = sync_ops<1>(ws + WS_F2, grp * 16 + rk, rep, tag, l);
            drain();
            sync_arrive(so); sync_wait1<0>(so);
            bar();
            if (blockIdx.x == 0 && l == 0) *(u32*)(ws + WS_EPOCH) = tag;
            bar(); bar();
            publish_keys();
            select8<6>(ws, 28 + (rk & 3), l, smw);
        } else {
            drain();
            Fill f = fill_init(wd + (size_t)grp * 448 * D_ + rk * 64, D_ * 2, 448, l);
            bar(); bar(); bar();
            if (w8 == 5) fill<60>(f, DB8);
            bar(); bar();
            if (w8 == 5) { __builtin_amdgcn_s_sleep(1); fill<DEPTHD>(f, 14 - DB8); drain(); STAMP(3); }
        }
        bar();
        down_phase(ws, out, sm, w8, l, grp, rk, [&]() {} STARG);
    } else if (w8 == 4) {
        init_state();
        { const SyncOps so = sync_ops<2>(ws + WS_F1, p, rep, tag, l);
          bar(); drain();
          asm volatile("" :: "v"(cpf));
          sync_arrive(so); }
        { const SyncOps so = sync_ops<1>(ws + WS_F2, 0, rep, tag, l);
          for (u32 sp = 0; sp < SPIN_CAP; ++sp) { if (*(volatile LAS u32*)(smw + 2) == tag) break; __builtin_amdgcn_s_sleep(4); }
          sync_wait1<0>(so); }
        bar();
        bar(); bar();
        publish_keys();
        STAMP(5);
        if (rk < 14) select8<0>(ws, (grp * 14 + rk) % 28, l, smw); else select8<6>(ws, 28 + (rk & 3), l, smw);
        STAMP(7);
        bar();
    } else if (w8 == 5) {
        { const u32 to = ((grp + 8 * (16 * rk + (l >> 3))) * DF_ + (l & 7) * 64) * 2, to2 = l < 16 ? ((grp + 8 * (16 * rk + l)) * DF_ + 512) * 2 : 0x7ffffff0u;
          __builtin_amdgcn_s_sleep(LDELAY);
          touch(rFr, to, 0, L_JUNK); touch(rFi, to, 0, L_JUNK); touch(rFr, to, 64 * DF_ * 2, L_JUNK); touch(rFi, to, 64 * DF_ * 2, L_JUNK);
          touch(rFr, to2, 0, L_JUNK); touch(rFi, to2, 0, L_JUNK); }
        init_state();
        const u16* wbase = ((rk & 1) ? wu : wg) + grp * 448;
        const int slow = (3 - (int)((u64)wbase >> 7)) & 7;
        Fill f = fill_init(wbase + (rk < 14 ? (rk >> 1) : min(slow, 6)) * 64, F_ * 2, 1024, l);
        const SyncOps so = sync_ops<2>(ws + WS_F1, 0, rep, tag, l);
        const int on = rk < 14 ? 1 : 0;
        auto assist = [&](int i0, int i1) {
            if (rk >= 14 && slow < 7)
#pragma unroll 1
                for (int i = i0; i < i1; ++i) { touch(f.r, l * (F_ * 2), i * 64 * F_ * 2, L_JUNK); asm volatile("s_waitcnt vmcnt(2)" ::: "memory"); }
        };
        if (l == 0) smw[2] = tag - 1u;
        STAMP(1);
        bar(); bar();
        __builtin_amdgcn_s_sleep(1);
        fill<DEPTH>(f, on * (NS1 / 4));
        assist(AS0, 16);
        STAMP(2);
        if (NS1 < 128 && rk < 14 && (rk >> 1) != slow) { sync_wait2<4>(so); __builtin_amdgcn_s_sleep(TS2); }
        STAMP(3);
        fill<DEPTH>(f, on * ((NS2 - NS1) / 4));
        {
          const v4u rD = mkrsrc(wd + (size_t)grp * 448 * D_, 448u * D_ * 2); const u32 to = (4 * rk + (l >> 4)) * (D_ * 2) + (l & 15) * 128;
#pragma unroll 1
          for (int i = 0; i < ((rk >> 1) == slow ? 0 : NDT); ++i) { touch(rD, to, i * 64 * D_ * 2, L_JUNK); asm volatile("s_waitcnt vmcnt(3)" ::: "memory"); } }
        if (l == 0) *(volatile LAS u32*)(smw + 2) = tag;
        STAMP(4);
        bar(); bar(); bar(); bar();
        __builtin_amdgcn_s_sleep(1);
        fill<DEPTH>(f, on * ((128 - NS2) / 4));
        drain();
        asm volatile("" :: "v"(cpf));
        STAMP(8);
        if (l == 0) *(volatile LAS u32*)smw = 1u;
        asm volatile("s_waitcnt lgkmcnt(0)" ::: "memory");
    } else { drain(); asm volatile("" :: "v"(cpf)); return; }
    STAMP_OUT;
    }
}
}

extern "C" void kernel_launch(void* const* d_in, const int* in_sizes, int n_in, void* d_out, int out_size, void* d_ws, size_t ws_size, hipStream_t stream) {
    (void)in_sizes; (void)n_in; (void)out_size; (void)ws_size;
    (void)hipFuncSetAttribute((const void*)fused, hipFuncAttributeMaxDynamicSharedMemorySize, L_TOTAL);
    fused<<<256, 512, L_TOTAL, stream>>>((const u16*)d_in[0], (const u16*)d_in[1], (const u16*)d_in[2], (const u16*)d_in[4],
                                         (const u16*)d_in[5], (const u16*)d_in[6], (const u16*)d_in[7], (u16*)d_out, (char*)d_ws);
}
```
